```python
import torch

def _softmax(x, axis=-1):
    return torch.softmax(x, dim=axis)
def _gelu(x):
    return torch.nn.functional.gelu(x, approximate="tanh")
def _silu(x):
    return torch.nn.functional.silu(x)
def _sigmoid(x):
    return torch.sigmoid(x)
def _layernorm(x, axis=-1, eps=1e-5):
    m = x.mean(dim=axis, keepdim=True)
    v = x.var(dim=axis, keepdim=True, unbiased=False)
    return (x - m) / torch.sqrt(v + eps)
def _scatter_add(tgt, idx, src, axis=0):
    return tgt.clone().index_add_(axis, idx, src)
def _segment_sum(src, seg, n):
    out = torch.zeros((n,) + src.shape[1:], dtype=src.dtype)
    return out.index_add_(0, seg, src)
def _pad(x, pw):
    flat = tuple(v for pair in reversed(pw) for v in pair)
    return torch.nn.functional.pad(x, flat)

kernel_name = 'parallel_sum__ssm_conv_s5__dense_activation_squared_relu__prompt'

def setup_inputs():

    _w = (lambda a: (lambda t: t.to(torch.bfloat16) if t.is_floating_point() else t)(torch.as_tensor(a)))
    def _A_setup():
        B, S, D = 1, 2048, 512
        P = D
        Lambda_re = -0.5 * (lambda _x: torch.exp(torch.as_tensor(_x)))((lambda *s: torch.randn(*s, dtype=torch.bfloat16) * 0.8)(P))
        Lambda_im = (lambda *s: torch.randn(*s, dtype=torch.bfloat16) * 0.8)(P) * 1.0
        B_re = (lambda *s: torch.randn(*s, dtype=torch.bfloat16) * 1.6)(P, D) * (D ** -0.5)
        B_im = (lambda *s: torch.randn(*s, dtype=torch.bfloat16) * 1.6)(P, D) * (D ** -0.5)
        C_re = (lambda *s: torch.randn(*s, dtype=torch.bfloat16) * 1.6)(D, P) * (P ** -0.5)
        C_im = (lambda *s: torch.randn(*s, dtype=torch.bfloat16) * 1.6)(D, P) * (P ** -0.5)
        log_dt = (lambda *s: torch.randn(*s, dtype=torch.bfloat16) * 0.8)(P) * 0.3 - 2.0
        Dskip = (lambda *s: torch.randn(*s, dtype=torch.bfloat16) * 0.4)(D)
        return {
            "x": _w((lambda *s: torch.randn(*s, dtype=torch.bfloat16) * 0.05)(B, S, D)),
            "Lambda_re": _w(Lambda_re),
            "Lambda_im": _w(Lambda_im),
            "B_re": _w(B_re),
            "B_im": _w(B_im),
            "C_re": _w(C_re),
            "C_im": _w(C_im),
            "log_dt": _w(log_dt),
            "Dskip": _w(Dskip),
        }
    _A_in = _A_setup()

    def _B_setup():
        B, S, D, F = 1, 2048, 512, 1792
        return {
            "x": _w((lambda *s: torch.randn(*s, dtype=torch.bfloat16) * 0.05)(B, S, D)),
            "w1": _w((lambda *s: torch.randn(*s, dtype=torch.bfloat16) * 0.05)(D, F)),
            "b1": _w((lambda *s: torch.randn(*s, dtype=torch.bfloat16) * 0.2)(F)),
            "w2": _w((lambda *s: torch.randn(*s, dtype=torch.bfloat16) * 0.05)(F, D)),
            "b2": _w((lambda *s: torch.randn(*s, dtype=torch.bfloat16) * 1.6)(D)),
        }
    _B_in = _B_setup()

    return {
        "A_x": _A_in["x"],
        "A_Lambda_re": _A_in["Lambda_re"],
        "A_Lambda_im": _A_in["Lambda_im"],
        "A_B_re": _A_in["B_re"],
        "A_B_im": _A_in["B_im"],
        "A_C_re": _A_in["C_re"],
        "A_C_im": _A_in["C_im"],
        "A_log_dt": _A_in["log_dt"],
        "A_Dskip": _A_in["Dskip"],
        "B_x": _B_in["x"],
        "B_w1": _B_in["w1"],
        "B_b1": _B_in["b1"],
        "B_w2": _B_in["w2"],
        "B_b2": _B_in["b2"],
    }

def reference(A_x, A_Lambda_re, A_Lambda_im, A_B_re, A_B_im, A_C_re, A_C_im, A_log_dt, A_Dskip, B_x, B_w1, B_b1, B_w2, B_b2):
    def _A_ref(x, Lambda_re, Lambda_im, B_re, B_im, C_re, C_im, log_dt, Dskip):
        Bsz, L, H = x.shape
        Lam = Lambda_re + 1j * Lambda_im
        Bmat = B_re + 1j * B_im
        Cmat = C_re + 1j * C_im
        dt = torch.exp(log_dt)
        Lam_bar = torch.exp(dt * Lam)
        B_bar = ((Lam_bar - 1.0) / Lam)[:, None] * Bmat
        Bu = torch.einsum("bld,pd->blp", x + 0j, B_bar)
        h = torch.zeros_like(Bu[:, 0, :])
        hs = []
        for t in range(L):
            h = Lam_bar[None, :] * h + Bu[:, t, :]
            hs.append(h)
        hseq = torch.stack(hs, 1)
        y = 2.0 * torch.einsum("blp,dp->bld", hseq, Cmat).real + x * Dskip[None, None, :]
        return y

    def _B_ref(x, w1, b1, w2, b2):
        h = torch.einsum("bsd,df->bsf", x, w1) + b1
        zero = torch.zeros_like(h)
        r = torch.where(h > zero, h, zero)
        h = r * r
        out = torch.einsum("bsf,fd->bsd", h, w2) + b2
        return out

    x = A_x
    xn = _layernorm(x, axis=-1)
    return x + _A_ref(xn, A_Lambda_re, A_Lambda_im, A_B_re, A_B_im, A_C_re, A_C_im, A_log_dt, A_Dskip) + _B_ref(xn, B_w1, B_b1, B_w2, B_b2)
```

```cpp
#include <hip/hip_runtime.h>
#include <hip/hip_bf16.h>
typedef unsigned short u16;
__device__ __forceinline__ float bf2f(u16 v){ return __uint_as_float(((unsigned)v)<<16); }
__device__ __forceinline__ u16 f2bf(float f){ unsigned u=__float_as_uint(f); u = u + 0x7FFF + ((u>>16)&1); return (u16)(u>>16); }
__device__ __forceinline__ float rbf(float f){ return bf2f(f2bf(f)); }
constexpr int L=2048, D=512, P=512, F=1792;
__global__ void k_ln(const u16* x, float* xn){
  int row=blockIdx.x; int t=threadIdx.x;
  float v[8]; float s=0;
  for(int i=0;i<8;i++){ v[i]=bf2f(x[row*D+t*8+i]); s+=v[i]; }
  for(int o=32;o;o>>=1) s+=__shfl_xor(s,o);
  float m=s/D; float q=0; for(int i=0;i<8;i++){ float d=v[i]-m; q+=d*d; }
  for(int o=32;o;o>>=1) q+=__shfl_xor(q,o);
  float var=rbf(q/D); float mb=rbf(m); float sd=rbf(sqrtf(rbf(var+1e-5f)));
  for(int i=0;i<8;i++) xn[row*D+t*8+i]=rbf(rbf(v[i]-mb)/sd);
}
__global__ void k_g1(const float* xn,const u16* Bre,const u16* Bim,const u16* w1,const u16* b1,float* bu,float* h){
  int row=blockIdx.y; int c=blockIdx.x*256+threadIdx.x;
  float acc=0;
  if(c<512){ for(int k=0;k<D;k++) acc+=xn[row*D+k]*bf2f(Bre[c*D+k]); bu[row*1024+c]=acc; }
  else if(c<1024){ int p=c-512; for(int k=0;k<D;k++) acc+=xn[row*D+k]*bf2f(Bim[p*D+k]); bu[row*1024+c]=acc; }
  else { int f=c-1024; for(int k=0;k<D;k++) acc+=xn[row*D+k]*bf2f(w1[k*F+f]); float hh=rbf(rbf(acc)+bf2f(b1[f])); hh=hh>0?hh:0; h[row*F+f]=rbf(hh*hh);}
}
__global__ void k_scan(float* bu,const u16* Lre,const u16* Lim,const u16* ldt){
  int p=blockIdx.x*64+threadIdx.x;
  float lr=bf2f(Lre[p]), li=bf2f(Lim[p]); float dt=rbf(expf(bf2f(ldt[p])));
  float e=expf(dt*lr); float ar=e*cosf(dt*li), ai=e*sinf(dt*li);
  float nr=ar-1.f, ni=ai; float den=lr*lr+li*li; float cr=(nr*lr+ni*li)/den, ci=(ni*lr-nr*li)/den;
  float hr=0,hi=0;
  for(int t=0;t<L;t++){ float ur=bu[t*1024+p], ui=bu[t*1024+512+p]; float br=cr*ur-ci*ui, bi=cr*ui+ci*ur;
    float nhr=ar*hr-ai*hi+br, nhi=ar*hi+ai*hr+bi; hr=nhr; hi=nhi; bu[t*1024+p]=hr; bu[t*1024+512+p]=hi; }
}
__global__ void k_g2(const u16* x,const float* xn,const float* hs,const float* h,const u16* Cre,const u16* Cim,const u16* w2,const u16* b2,const u16* Dsk,float* out){
  int row=blockIdx.y; int d=blockIdx.x*256+threadIdx.x;
  float a=0; for(int p=0;p<P;p++) a+=hs[row*1024+p]*bf2f(Cre[d*P+p])-hs[row*1024+512+p]*bf2f(Cim[d*P+p]);
  float b=0; for(int f=0;f<F;f++) b+=h[row*F+f]*bf2f(w2[f*D+d]);
  float y=2.f*a+rbf(xn[row*D+d]*bf2f(Dsk[d]));
  out[row*D+d]=bf2f(x[row*D+d])+y+rbf(rbf(b)+bf2f(b2[d]));
}
extern "C" void kernel_launch(void* const* d_in, const int* in_sizes, int n_in, void* d_out, int out_size, void* d_ws, size_t ws_size, hipStream_t stream){
  const u16* x=(const u16*)d_in[0]; const u16* Lre=(const u16*)d_in[1]; const u16* Lim=(const u16*)d_in[2];
  const u16* Bre=(const u16*)d_in[3]; const u16* Bim=(const u16*)d_in[4]; const u16* Cre=(const u16*)d_in[5]; const u16* Cim=(const u16*)d_in[6];
  const u16* ldt=(const u16*)d_in[7]; const u16* Dsk=(const u16*)d_in[8]; const u16* w1=(const u16*)d_in[10]; const u16* b1=(const u16*)d_in[11]; const u16* w2=(const u16*)d_in[12]; const u16* b2=(const u16*)d_in[13];
  float* xn=(float*)d_ws; float* bu=xn+L*D; float* h=bu+L*1024;
  k_ln<<<L,64,0,stream>>>(x,xn);
  k_g1<<<dim3(11,L),256,0,stream>>>(xn,Bre,Bim,w1,b1,bu,h);
  k_scan<<<8,64,0,stream>>>(bu,Lre,Lim,ldt);
  k_g2<<<dim3(2,L),256,0,stream>>>(x,xn,bu,h,Cre,Cim,w2,b2,Dsk,(float*)d_out);
}
```

```cpp
#include <hip/hip_runtime.h>
#include <type_traits>

typedef unsigned short u16;
typedef unsigned u32;
typedef unsigned long long u64;
typedef __attribute__((ext_vector_type(8))) __bf16 bf16x8;
typedef __attribute__((ext_vector_type(2))) __bf16 bf16x2;
typedef __attribute__((ext_vector_type(16))) float f32x16;
typedef __attribute__((ext_vector_type(4))) float f32x4;
typedef __attribute__((ext_vector_type(2))) float f32x2;
typedef __attribute__((ext_vector_type(4))) u32 u32x4;
typedef __attribute__((ext_vector_type(2))) u32 u32x2;
typedef __attribute__((ext_vector_type(4))) short s16x4;
typedef __amdgpu_buffer_rsrc_t rsrc_t;
#define DI __device__ __forceinline__
#define AS3 __attribute__((address_space(3)))

constexpr int LDS_BYTES = 163840;
constexpr u32 WS_H = 0;
constexpr u32 WS_CARRY = 64u * 176u * 1024u;
constexpr u32 WS_FLAG = WS_CARRY + 131072;
constexpr u32 WS_NONR = WS_FLAG + 2048;
constexpr u32 WS_NONJ = WS_NONR + 2048;
constexpr u32 WS_USED = WS_NONJ + 2048;
constexpr u32 L_MISC = 147456;
constexpr u32 L_CSS = L_MISC;
constexpr u32 L_CSM = L_CSS + 2048;
constexpr u32 L_E = L_CSM + 2048;
constexpr u32 L_CAR = L_E + 2048;
constexpr u32 L_STM = L_CAR + 4096;
constexpr u32 L_STR = L_STM + 512;

__shared__ __attribute__((aligned(1024))) char smem[LDS_BYTES];
template <class T> DI AS3 T* lp(u32 off) { return (AS3 T*)((AS3 char*)smem + off); }

template <int I, int N, class F> DI void sfor(F&& f) {
  if constexpr (I < N) { f(std::integral_constant<int, I>{}); sfor<I + 1, N>(f); }
}
DI f32x16 mfma(u32x4 a, u32x4 b, f32x16 c) {
  return __builtin_amdgcn_mfma_f32_32x32x16_bf16(__builtin_bit_cast(bf16x8, a), __builtin_bit_cast(bf16x8, b), c, 0, 0, 0);
}
DI float dot2(u32 a, u32 b, float c) {
  return __builtin_amdgcn_fdot2_f32_bf16(__builtin_bit_cast(bf16x2, a), __builtin_bit_cast(bf16x2, b), c, false);
}
DI u32 pkbf(float lo, float hi) { bf16x2 v = {(__bf16)lo, (__bf16)hi}; return __builtin_bit_cast(u32, v); }
DI float rbf(float f) { return (float)(__bf16)f; }
DI float bf2f(u32 v) { return __uint_as_float(v << 16); }
DI u32x4 trfrag(u32 a0, u32 a1) {
  s16x4 lo = __builtin_amdgcn_ds_read_tr16_b64_v4i16(lp<s16x4>(a0));
  s16x4 hi = __builtin_amdgcn_ds_read_tr16_b64_v4i16(lp<s16x4>(a1));
  u32x2 l = __builtin_bit_cast(u32x2, lo), h = __builtin_bit_cast(u32x2, hi);
  return u32x4{l.x, l.y, h.x, h.y};
}
template <int N> DI void wait_bar() { asm volatile("s_waitcnt vmcnt(%0) lgkmcnt(0)\n\ts_barrier" ::"n"(N) : "memory"); }
DI void dma4(u32 m0, u32 v0, u32 v1, u32 v2, u32 v3, rsrc_t rs, u32 so) {
  asm volatile("s_mov_b32 m0, %0\n\ts_nop 0\n\t"
               "buffer_load_dwordx4 %1, %5, %6 offen lds\n\t"
               "buffer_load_dwordx4 %2, %5, %6 offen offset:1024 lds\n\t"
               "buffer_load_dwordx4 %3, %5, %6 offen offset:2048 lds\n\t"
               "buffer_load_dwordx4 %4, %5, %6 offen offset:3072 lds"
               ::"s"(m0), "v"(v0), "v"(v1), "v"(v2), "v"(v3), "s"(rs), "s"(so) : "memory");
}
DI void dma2(u32 m0, u32 v0, u32 v1, rsrc_t rs, u32 so) {
  asm volatile("s_mov_b32 m0, %0\n\ts_nop 0\n\t"
               "buffer_load_dwordx4 %1, %3, %4 offen lds\n\t"
               "buffer_load_dwordx4 %2, %3, %4 offen offset:1024 lds"
               ::"s"(m0), "v"(v0), "v"(v1), "s"(rs), "s"(so) : "memory");
}
DI u32 ld_u16(u32 vo, rsrc_t rs, u32 so) { u32 r; asm volatile("buffer_load_ushort %0, %1, %2, %3 offen" : "=v"(r) : "v"(vo), "s"(rs), "s"(so) : "memory"); return r; }
DI u32x2 ld_b64_dev(u32 vo, rsrc_t rs, u32 so) { u32x2 r; asm volatile("buffer_load_dwordx2 %0, %1, %2, %3 offen sc1" : "=v"(r) : "v"(vo), "s"(rs), "s"(so) : "memory"); return r; }
DI u32x4 ld_b128_dev(u32 vo, rsrc_t rs, u32 so) { u32x4 r; asm volatile("buffer_load_dwordx4 %0, %1, %2, %3 offen sc1" : "=v"(r) : "v"(vo), "s"(rs), "s"(so) : "memory"); return r; }
DI u32x4 ld_b128_dev1k(u32 vo, rsrc_t rs, u32 so) { u32x4 r; asm volatile("buffer_load_dwordx4 %0, %1, %2, %3 offen offset:1024 sc1" : "=v"(r) : "v"(vo), "s"(rs), "s"(so) : "memory"); return r; }
DI rsrc_t mkrs(const void* p, u32 bytes) { return __builtin_amdgcn_make_buffer_rsrc((void*)p, 0, bytes, 0x00020000); }
DI float sum_halves(float v) {
  auto r = __builtin_amdgcn_permlane32_swap(__float_as_uint(v), __float_as_uint(v), false, false);
  return __uint_as_float(r[0]) + __uint_as_float(r[1]);
}
DI u32x4 split3(float v, u32 e3) {
  u32 h = __float_as_uint(v) & 0xffff0000u;
  float r1 = v - __uint_as_float(h);
  u32 m = __float_as_uint(r1) & 0xffff0000u;
  float r2 = r1 - __uint_as_float(m);
  return u32x4{(h >> 16) | m, (__float_as_uint(r2) >> 16) | (e3 << 16), 0u, 0u};
}
constexpr int SPIN_CAP = 1 << 17;

#define ST(i)

__global__ __launch_bounds__(256, 1) void s5_mlp_fused(
    const u16* __restrict__ px, const u16* __restrict__ plre, const u16* __restrict__ plim, const u16* __restrict__ pbre,
    const u16* __restrict__ pbim, const u16* __restrict__ pcre, const u16* __restrict__ pcim, const u16* __restrict__ pldt,
    const u16* __restrict__ pdsk, const u16* __restrict__ pw1, const u16* __restrict__ pb1, const u16* __restrict__ pw2,
    const u16* __restrict__ pb2, float* __restrict__ pout, char* __restrict__ pws) {
  ST(0);
  const int tid = threadIdx.x, lane = tid & 63;
  const int w = __builtin_amdgcn_readfirstlane(tid >> 6);
  const int bid = blockIdx.x, xcd = bid & 7, idx = bid >> 3;
  const int r = (xcd >> 1) * 4 + (idx >> 3), j = (xcd & 1) * 8 + (idx & 7);
  const int ghalf = xcd & 1, gc = idx & 7;
  const bool odd = j & 1;
  const int NM = odd ? 6 : 8;
  const int u0 = 7 * (j >> 1) + (odd ? 4 : 0);
  const int l31 = lane & 31, hh = lane >> 5;

  const rsrc_t rx = mkrs(px, 2048 * 512 * 2);
  const rsrc_t rb = mkrs(w < 2 ? pbre : pbim, 512 * 512 * 2);
  const rsrc_t rw1 = mkrs(pw1, 512 * 1792 * 2);
  const rsrc_t rw2 = mkrs(pw2, 1792 * 512 * 2);
  const rsrc_t rc = mkrs((w & 1) ? pcim : pcre, 512 * 512 * 2);
  const rsrc_t rws = mkrs(pws, WS_USED);

  const int rho = lane >> 3, sl = lane & 7;
  u32 vx[4], vb[2];
  sfor<0, 4>([&](auto q) { vx[q] = (16 * (rho >> 2) + (rho & 3) + 3 * q) * 1024 + 16 * (sl ^ ((rho >> 1) | ((q & 1) << 2))); });
  sfor<0, 2>([&](auto q) { vb[q] = (7 * q + rho) * 1024 + 16 * (sl ^ ((rho >> 1) | (q << 2))); });
  const u32 sox = (128 * r + 32 * w) * 1024, sob = (32 * j + 16 * (w & 1)) * 1024;
  auto issue_chunk = [&](u32 c, u32 slot) {
    dma4(slot + w * 4096, vx[0], vx[1], vx[2], vx[3], rx, sox + 128 * c);
    dma2(slot + 16384 + w * 2048, vb[0], vb[1], rb, sob + 128 * c);
  };
  issue_chunk(0, 0);
  u32 qlre = ld_u16(l31 * 2, mkrs(plre, 1024), 64 * j);
  u32 qlim = ld_u16(l31 * 2, mkrs(plim, 1024), 64 * j);
  u32 qldt = ld_u16(l31 * 2, mkrs(pldt, 1024), 64 * j);
  u32x2 nonr = ld_b64_dev(0, rws, WS_NONR + 128 * r);
  u32x2 nonj = ld_b64_dev(0, rws, WS_NONJ + 128 * j);
  u32 qb1[4];
  {
    const rsrc_t rb1 = mkrs(pb1, 1792 * 2);
    sfor<0, 4>([&](auto t) { qb1[t] = ld_u16(l31 * 2, rb1, 64 * (u0 + t)); });
  }
  u32 qdsk = ld_u16(l31 * 2, mkrs(pdsk, 1024), 128 * gc + 64 * (w >> 1));
  u32 qb2 = ld_u16(l31 * 2, mkrs(pb2, 1024), 128 * gc + 64 * (w >> 1));
  u32 pf;
  {
    u64 pc; u32 len;
    asm volatile("s_getpc_b64 %0\n.Lcode_a:\n\ts_mov_b32 %1, .Lcode_b-.Lcode_a" : "=s"(pc), "=s"(len));
    const rsrc_t rcode = mkrs((const void*)(pc & ~63ull), len);
    asm volatile("buffer_load_dword %0, %1, %2, %3 offen" : "=v"(pf) : "v"(lane * 64), "s"(rcode), "s"((u32)((idx & 3) * 4 + w) * 4096) : "memory");
  }
#pragma unroll 1
  for (u32 c = 1; c < 6; ++c) issue_chunk(c, c * 24576);

  u32 vw1[4], vw2[4], vc[4];
  sfor<0, 4>([&](auto q) {
    vw1[q] = (lane >> 2) * 3584 + 16 * (lane & 3) + q * (16 * 3584 - 1024);
    vw2[q] = rho * 1024 + 16 * (sl ^ ((rho & 2) << 1)) + q * 7168;
    vc[q] = (lane >> 2) * 1024 + 16 * ((lane & 3) ^ (lane >> 4)) + q * 15360;
  });
  auto uslot = [](int n) -> u32 { return ((n + 3) % 9) * 16384; };
  auto issue_w1 = [&](int n) {
    dma4(uslot(n) + w * 4096, vw1[0], vw1[1], vw1[2], vw1[3], rw1, (256 * (n & 1) + 64 * w) * 3584 + 64 * (u0 + (n >> 1)));
  };
  auto unit_of = [&](int stg, int pi) -> int {
    if (stg < 12) { int jj = 4 * (stg & 3) + pi; return 7 * (jj >> 1) + 4 * (jj & 1) + (stg >> 2); }
    return 7 * (4 * (stg - 12) + pi) + 3;
  };
  auto issue_w2 = [&](int stg, u32 slot) {
    dma4(slot + w * 4096, vw2[0], vw2[1], vw2[2], vw2[3], rw2, unit_of(stg, w) * 32768 + 128 * gc);
  };
  auto issue_c = [&](int stg, u32 slot) {
    dma4(slot + w * 4096, vc[0], vc[1], vc[2], vc[3], rc, gc * 65536 + 64 * (2 * (stg - 14) + (w >> 1)));
  };
  auto issue_unit = [&](auto n) {
    if constexpr (n < 6) issue_w1(n);
    else if constexpr (n < 8) { if (odd) issue_w2(n - 6, uslot(n)); else issue_w1(n); }
    else issue_w2(n - NM, uslot(n));
  };

  u32 a0[4];
  sfor<0, 4>([&](auto q) { a0[q] = l31 * 128 + 16 * ((2 * ((q + w) & 3) + hh) ^ ((l31 >> 1) & 7)); });
  u32x4 X[32];
  f32x16 aR = {}, aI = {};
  float s1[2] = {0.f, 0.f}, s2[2] = {0.f, 0.f}, csR = 0.f, csI = 0.f;
  const u32 ONES = 0x3f803f80u;
  float ar, ai, cr, ci;
  ST(1);
  sfor<0, 8>([&](auto c) {
    constexpr int cnt[8] = {42, 24, 24, 24, 22, 24, 22, 24};
    if constexpr (c == 1)
      asm volatile("s_waitcnt vmcnt(24) lgkmcnt(0)\n\ts_barrier"
                   : "+v"(qlre), "+v"(qlim), "+v"(qldt), "+v"(nonr), "+v"(nonj), "+v"(qb1[0]), "+v"(qb1[1]), "+v"(qb1[2]), "+v"(qb1[3]),
                     "+v"(qdsk), "+v"(qb2), "+v"(pf)::"memory");
    else wait_bar<cnt[c]>();
    if constexpr (c == 0) ST(2);
    if constexpr (c == 1) issue_chunk(6, 0);
    if constexpr (c == 2) issue_chunk(7, 24576);
    if constexpr (c == 3) issue_unit(std::integral_constant<int, 0>{});
    if constexpr (c == 4) { issue_unit(std::integral_constant<int, 1>{}); issue_unit(std::integral_constant<int, 2>{}); }
    if constexpr (c == 5) issue_unit(std::integral_constant<int, 3>{});
    if constexpr (c == 6) { issue_unit(std::integral_constant<int, 4>{}); issue_unit(std::integral_constant<int, 5>{}); }
    if constexpr (c == 7) issue_unit(std::integral_constant<int, 6>{});
    constexpr u32 slot = (c % 6) * 24576;
    u32x4 bR[4], bI[4];
    sfor<0, 4>([&](auto q) {
      X[4 * c + q] = *lp<u32x4>(a0[q] + w * 4096 + slot);
      bR[q] = *lp<u32x4>(a0[q] + slot + 16384);
      bI[q] = *lp<u32x4>(a0[q] + slot + 16384 + 4096);
    });
    sfor<0, 4>([&](auto q) {
      sfor<0, 4>([&](auto e) {
        s1[e & 1] = dot2(X[4 * c + q][+e], ONES, s1[e & 1]);
        s2[e & 1] = dot2(X[4 * c + q][+e], X[4 * c + q][+e], s2[e & 1]);
      });
    });
    sfor<0, 4>([&](auto e) { csR = dot2(bR[0][+e], ONES, csR); csI = dot2(bI[0][+e], ONES, csI); });
    asm volatile("" : "+v"(s1[0]), "+v"(s1[1]), "+v"(s2[0]), "+v"(s2[1]), "+v"(csR), "+v"(csI));
    __builtin_amdgcn_sched_barrier(0);
    sfor<0, 4>([&](auto q) { aR = mfma(X[4 * c + q], bR[q], aR); aI = mfma(X[4 * c + q], bI[q], aI); });
    if constexpr (c == 1) {
      const float lr = bf2f(qlre), li = bf2f(qlim), dt = rbf(__expf(bf2f(qldt)));
      const float zr = dt * lr, zi = dt * li;
      const float e = __expf(zr);
      const float kf = rintf(zi * 0.63661977236758134308f);
      float y = fmaf(kf, -1.57079637050628662109375f, zi);
      y = fmaf(kf, 4.37113900018624283e-8f, y);
      const float y2 = y * y;
      float sp = fmaf(y2, 2.7525562e-6f, -1.9840874e-4f); sp = fmaf(sp, y2, 8.3333310e-3f); sp = fmaf(sp, y2, -1.6666667e-1f);
      const float sn = fmaf(sp * y2, y, y);
      float cp = fmaf(y2, -2.6051615e-7f, 2.4760495e-5f); cp = fmaf(cp, y2, -1.3888378e-3f); cp = fmaf(cp, y2, 4.1666638e-2f); cp = fmaf(cp, y2, -0.5f);
      const float cs = fmaf(cp, y2, 1.0f);
      const int k = (int)kf;
      float sv = (k & 1) ? cs : sn, cv = (k & 1) ? sn : cs;
      sv = (k & 2) ? -sv : sv;
      cv = ((k + 1) & 2) ? -cv : cv;
      ar = e * cv; const float aim = e * sv;
      const float nr = ar - 1.f, inv = 2.f / (lr * lr + li * li);
      cr = (nr * lr + aim * li) * inv; ci = (aim * lr - nr * li) * inv;
      ai = -aim;
      asm volatile("" : "+v"(ar), "+v"(ai), "+v"(cr), "+v"(ci));
    }
    __builtin_amdgcn_sched_barrier(0);
  });
  ST(3);
  const float mean = sum_halves(s1[0] + s1[1]) * (1.f / 512.f);
  const float var = fmaxf(sum_halves(s2[0] + s2[1]) * (1.f / 512.f) - mean * mean, 0.f);
  const float mb = rbf(mean), sd = rbf(sqrtf(rbf(rbf(var) + 1e-5f))), rs = 1.f / sd;
  const u32 nmb = __float_as_uint(-mb) >> 16;
  const u32x4 XS = {nmb | (nmb << 16), nmb | (__float_as_uint(sd) & 0xffff0000u), 0u, 0u};
  {
    const int tau = 16 * ((l31 >> 2) & 1) + 4 * (l31 >> 3) + (l31 & 3);
    *lp<float>((hh ? L_STR : L_STM) + (32 * w + tau) * 4) = hh ? rs : mb;
    *lp<float>(L_CSS + lane * 16 + w * 4) = csR;
    *lp<float>(L_CSS + 1024 + lane * 16 + w * 4) = csI;
  }
  const u32 tagj = __builtin_amdgcn_readfirstlane(nonj.x) + 1;
  const u32 nrlo = __builtin_amdgcn_readfirstlane(nonr.x), nrhi = __builtin_amdgcn_readfirstlane(nonr.y) + 1;
  auto publish = [&](u32 level) {
    if (w == 0 && lane == 0) __builtin_amdgcn_raw_buffer_store_b64(u32x2{nrlo + level, nrhi}, rws, 8 * j, WS_FLAG + 128 * r, 16);
  };

  u32 at[4];
  sfor<0, 4>([&](auto q) {
    at[q] = ((q + w) & 3) * 1024 + (8 * hh + ((lane & 15) >> 2)) * 64 + ((lane >> 4) & 1) * 32 + (lane & 3) * 8;
  });
  f32x16 am[2];
  float csM = 0.f;
  const u32 soh = (4 * r + w) * 176 * 1024;
  auto epilogue_m = [&](auto T) {
    const f32x4 p = *lp<f32x4>(L_CSM + (T & 1) * 1024 + lane * 16);
    const u32x4 A = split3((p[0] + p[1]) + (p[2] + p[3]), hh ? 0u : qb1[T]);
    const f32x16 acc = mfma(A, XS, am[T & 1]);
    u32 o[8];
    sfor<0, 8>([&](auto e) {
      float v0 = fmaxf(acc[2 * e] * rs, 0.f), v1 = fmaxf(acc[2 * e + 1] * rs, 0.f);
      o[e] = pkbf(v0 * v0, v1 * v1);
    });
    __builtin_amdgcn_raw_buffer_store_b128(u32x4{o[0], o[1], o[2], o[3]}, rws, lane * 16, soh + (u0 + T) * 2048, 16);
    __builtin_amdgcn_raw_buffer_store_b128(u32x4{o[4], o[5], o[6], o[7]}, rws, lane * 16 + 1024, soh + (u0 + T) * 2048, 16);
  };
  float vR[16], vI[16];
  float a16r, a16i;
  u32x4 g0, g1;
  const int rp = 4 * w + 2 * hh;
  const u32 vcar = (rp * 16 + j) * 512 + l31 * 16;
  auto unit_m = [&](auto n) {
    constexpr int T = n >> 1, hf = n & 1;
    if constexpr (n == 0) wait_bar<24>();
    else if constexpr (n == 4 || n == 6) wait_bar<4>();
    else wait_bar<28>();
    ST(4 + n);
    if constexpr (n == 4) publish(1);
    if constexpr (n == 6) publish(2);
    if constexpr (n == 0) { issue_unit(std::integral_constant<int, 7>{}); issue_unit(std::integral_constant<int, 8>{}); }
    else issue_unit(std::integral_constant<int, n + 8>{});
    if constexpr (n == 5 || n == 7) {
      if (n == NM - 1) { g0 = ld_b128_dev(vcar, rws, WS_CARRY); g1 = ld_b128_dev(vcar, rws, WS_CARRY + 8192); }
    }
    if constexpr (n == 0) {
      const f32x4 pr = *lp<f32x4>(L_CSS + lane * 16), pi = *lp<f32x4>(L_CSS + 1024 + lane * 16);
      aR = mfma(XS, split3((pr[0] + pr[1]) + (pr[2] + pr[3]), 0u), aR);
      aI = mfma(XS, split3((pi[0] + pi[1]) + (pi[2] + pi[3]), 0u), aI);
      float er = 0.f, ei = 0.f;
      sfor<0, 4>([&](auto g) {
        const f32x4 rr = *lp<f32x4>(L_STR + (32 * w + 16 * hh + 4 * g) * 4);
        sfor<0, 4>([&](auto e) {
          constexpr int t = 4 * g + e;
          vR[t] = rr[+e] * (cr * aR[t] - ci * aI[t]);
          vI[t] = -rr[+e] * (cr * aI[t] + ci * aR[t]);
          const float nr = fmaf(-ai, ei, fmaf(ar, er, vR[t])), ni = fmaf(ai, er, fmaf(ar, ei, vI[t]));
          er = nr; ei = ni;
        });
      });
      *lp<f32x2>(L_E + ((2 * w + hh) * 32 + l31) * 8) = f32x2{er, ei};
      a16r = ar; a16i = ai;
      sfor<0, 4>([&](auto) { const float x = a16r * a16r - a16i * a16i; a16i = 2.f * a16r * a16i; a16r = x; });
    }
    if constexpr (n == 1) {
      float gr = 0.f, gi = 0.f;
      sfor<0, 8>([&](auto s) {
        const f32x2 e = *lp<f32x2>(L_E + (s * 32 + l31) * 8);
        const float nr = fmaf(-a16i, gi, fmaf(a16r, gr, e[0])), ni = fmaf(a16i, gr, fmaf(a16r, gi, e[1]));
        gr = nr; gi = ni;
      });
      if (w == 0) __builtin_amdgcn_raw_buffer_store_b64(u32x2{__float_as_uint(hh ? gi : gr), tagj}, rws, l31 * 16 + hh * 8, WS_CARRY + (r * 16 + j) * 512, 16);
    }
    if constexpr (hf == 0 && T > 0) epilogue_m(std::integral_constant<int, T - 1>{});
    if constexpr (hf == 0) { am[T & 1] = f32x16{}; csM = 0.f; }
    u32x4 keep[4];
    sfor<0, 4>([&](auto cc) {
      sfor<0, 4>([&](auto q) {
        const u32 ad = at[q] + uslot(n) + cc * 4096;
        const u32x4 wf = trfrag(ad, ad + 256);
        if constexpr (q == 0) keep[cc] = wf;
        am[T & 1] = mfma(wf, X[16 * hf + 4 * cc + q], am[T & 1]);
      });
    });
    __builtin_amdgcn_sched_barrier(0);
    sfor<0, 4>([&](auto cc) { sfor<0, 4>([&](auto e) { csM = dot2(keep[cc][+e], ONES, csM); }); });
    if constexpr (hf == 1) *lp<float>(L_CSM + (T & 1) * 1024 + lane * 16 + w * 4) = csM;
  };
  sfor<0, 6>(unit_m);
  if (!odd) sfor<6, 8>(unit_m);
  asm volatile("s_waitcnt vmcnt(0)" : "+v"(g0), "+v"(g1)::"memory");
#pragma unroll 1
  for (int spin = 0; spin < SPIN_CAP; ++spin) {
    const bool ok = (rp >= r || (g0[1] == tagj && g0[3] == tagj)) && (rp + 1 >= r || (g1[1] == tagj && g1[3] == tagj));
    if (__all(ok)) break;
    __builtin_amdgcn_s_sleep(2);
    g0 = ld_b128_dev(vcar, rws, WS_CARRY); g1 = ld_b128_dev(vcar, rws, WS_CARRY + 8192);
    asm volatile("s_waitcnt vmcnt(0)" : "+v"(g0), "+v"(g1)::"memory");
  }
  *lp<u32x2>(L_CAR + (rp * 32 + l31) * 8) = u32x2{g0[0], g0[2]};
  *lp<u32x2>(L_CAR + (rp * 32 + 32 + l31) * 8) = u32x2{g1[0], g1[2]};
  wait_bar<0>();
  ST(12);
  publish(odd ? 2 : 3);
  if (r == 15 && w == 0 && lane == 0) __builtin_amdgcn_raw_buffer_store_b32(tagj, rws, 0, WS_NONJ + 128 * j, 16);
  const u32 vfl = (lane & 15) * 8, sfl = WS_FLAG + 128 * r;
  u32x2 fl = ld_b64_dev(vfl, rws, sfl);
  if (odd) epilogue_m(std::integral_constant<int, 2>{}); else epilogue_m(std::integral_constant<int, 3>{});
  {
    float br = a16r, bi = a16i;
    sfor<0, 3>([&](auto) { const float x = br * br - bi * bi; bi = 2.f * br * bi; br = x; });
    float gr = 0.f, gi = 0.f;
#pragma unroll 1
    for (int s = 0; s < r; ++s) {
      const f32x2 e = *lp<f32x2>(L_CAR + (s * 32 + l31) * 8);
      const float nr = fmaf(-bi, gi, fmaf(br, gr, e[0])), ni = fmaf(bi, gr, fmaf(br, gi, e[1]));
      gr = nr; gi = ni;
    }
    const int seg = 2 * w + hh;
    sfor<0, 7>([&](auto s) {
      const f32x2 e = *lp<f32x2>(L_E + (s * 32 + l31) * 8);
      const float nr = fmaf(-a16i, gi, fmaf(a16r, gr, e[0])), ni = fmaf(a16i, gr, fmaf(a16r, gi, e[1]));
      gr = s < seg ? nr : gr; gi = s < seg ? ni : gi;
    });
    sfor<0, 16>([&](auto t) {
      const float nr = fmaf(-ai, gi, fmaf(ar, gr, vR[t])), ni = fmaf(ai, gr, fmaf(ar, gi, vI[t]));
      gr = nr; gi = ni; vR[t] = nr; vI[t] = ni;
    });
    const u32 tb = (odd ? uslot(5) : uslot(7)) + w * 4096;
    sfor<0, 2>([&](auto e) {
      const u32 ad = tb + l31 * 64 + 16 * ((2 * hh + e) ^ ((l31 >> 1) & 3));
      *lp<u32x4>(ad) = u32x4{pkbf(vR[8 * e], vR[8 * e + 1]), pkbf(vR[8 * e + 2], vR[8 * e + 3]), pkbf(vR[8 * e + 4], vR[8 * e + 5]), pkbf(vR[8 * e + 6], vR[8 * e + 7])};
      *lp<u32x4>(ad + 2048) = u32x4{pkbf(vI[8 * e], vI[8 * e + 1]), pkbf(vI[8 * e + 2], vI[8 * e + 3]), pkbf(vI[8 * e + 4], vI[8 * e + 5]), pkbf(vI[8 * e + 6], vI[8 * e + 7])};
    });
    const int qq = (lane & 15) >> 2, pp = lane & 3, c4 = 2 * (pp & 1) + ((lane >> 4) & 1);
    const u32 r0 = tb + (8 * hh + qq) * 64 + 16 * (c4 ^ (qq >> 1)) + 8 * (pp >> 1);
    const u32 r1 = tb + (8 * hh + qq + 4) * 64 + 16 * (c4 ^ (2 + (qq >> 1))) + 8 * (pp >> 1);
    sfor<0, 4>([&](auto f) {
      const u32x4 fr = trfrag(r0 + f * 1024, r1 + f * 1024);
      __builtin_amdgcn_raw_buffer_store_b128(fr, rws, lane * 16 + f * 1024, soh + (112 + 4 * j) * 1024, 16);
    });
  }
  ST(13);

  const int gm = w & 1, kh = w >> 1;
  const u32 sog = (4 * r + 2 * ghalf + gm) * 176 * 1024;
  auto check = [&](u32 level) {
#pragma unroll 1
    for (int spin = 0; spin < SPIN_CAP; ++spin) {
      if (__all(fl.y == nrhi && (fl.x - nrlo - level) <= 7u - level)) break;
      __builtin_amdgcn_s_sleep(1);
      fl = ld_b64_dev(vfl, rws, sfl);
      asm volatile("s_waitcnt vmcnt(0)" : "+v"(fl)::"memory");
    }
  };
  u32x4 Hb[3][4];
  auto issue_h = [&](auto s) {
    u32 k0, k1;
    if constexpr (s < 14) { k0 = 2 * unit_of(s, 2 * kh); k1 = 2 * unit_of(s, 2 * kh + 1); }
    else { k0 = 112 + 4 * (2 * (s - 14) + kh); k1 = k0 + 2; }
    Hb[s % 3][0] = ld_b128_dev(lane * 16, rws, sog + k0 * 1024);
    Hb[s % 3][1] = ld_b128_dev1k(lane * 16, rws, sog + k0 * 1024);
    Hb[s % 3][2] = ld_b128_dev(lane * 16, rws, sog + k1 * 1024);
    Hb[s % 3][3] = ld_b128_dev1k(lane * 16, rws, sog + k1 * 1024);
  };
  asm volatile("s_waitcnt vmcnt(6)" : "+v"(fl)::"memory");
  check(1);
  ST(14);
  sfor<0, 3>(issue_h);
  u32 aw[2], ac[2];
  {
    const int qq = (lane & 15) >> 2;
    sfor<0, 2>([&](auto n) {
      aw[n] = kh * 8192 + (4 * hh + qq) * 128 + 64 * (n ^ (qq >> 1)) + 32 * ((lane >> 4) & 1) + 8 * (lane & 3);
      ac[n] = kh * 8192 + l31 * 64 + 16 * ((2 * n + hh) ^ ((l31 >> 2) & 3));
    });
  }
  f32x16 o0 = {}, o1 = {};
  u32 xe[16];
  const u32 vxe = hh * 16 * 1024 + l31 * 2, sxe = (128 * r + 32 * (2 * ghalf + gm)) * 1024 + 128 * gc + 64 * kh;
  sfor<0, 22>([&](auto s) {
    constexpr auto nops = [](int q) {
      return (q + 8 < 22 ? 4 : 0) + ((q == 0 || q == 4 || q == 8) ? 1 : 0) + (q == 14 ? 16 : 0);
    };
    constexpr int young = s == 0 ? 8 : s == 1 ? 4 + nops(0) + 4 : s == 2 ? nops(0) + 4 + nops(1) + 4
                        : nops(s - 2) + (s + 1 < 22 ? 4 : 0) + nops(s - 1) + (s + 2 < 22 ? 4 : 0);
    constexpr int yfl = nops(s > 0 ? s - 1 : 0) - 1 + 4;
    constexpr int N = (s == 1 || s == 5 || s == 9) ? yfl : young;
    asm volatile("s_waitcnt vmcnt(%5) lgkmcnt(0)\n\ts_barrier"
                 : "+v"(Hb[s % 3][0]), "+v"(Hb[s % 3][1]), "+v"(Hb[s % 3][2]), "+v"(Hb[s % 3][3]), "+v"(fl) : "n"(N) : "memory");
    if constexpr (s == 0) { publish(4); ST(15); }
    if constexpr (s == 7) ST(16);
    if constexpr (s == 14) ST(17);
    if constexpr (s == 1) check(2);
    if constexpr (s == 5) check(3);
    if constexpr (s == 9) {
      check(4);
      if (j == 0 && w == 0 && lane == 0) __builtin_amdgcn_raw_buffer_store_b32(nrlo + 8, rws, 0, WS_NONR + 128 * r, 16);
    }
    if constexpr (s == 0 || s == 4 || s == 8) fl = ld_b64_dev(vfl, rws, sfl);
    if constexpr (s + 8 < 14) issue_w2(s + 8, odd ? uslot(s + 14) : uslot(s + 16));
    else if constexpr (s + 8 < 22) issue_c(s + 8, odd ? uslot(s + 14) : uslot(s + 16));
    if constexpr (s == 14) sfor<0, 16>([&](auto t) { xe[t] = ld_u16(vxe, rx, sxe + t * 1024); });
    const u32 slot = odd ? uslot(s + 6) : uslot(s + 8);
    sfor<0, 4>([&](auto e) {
      u32x4 b0, b1;
      if constexpr (s < 14) {
        b0 = trfrag(aw[0] + slot + e * 2048, aw[0] + slot + e * 2048 + 1024);
        b1 = trfrag(aw[1] + slot + e * 2048, aw[1] + slot + e * 2048 + 1024);
      } else {
        b0 = *lp<u32x4>(ac[e & 1] + slot + (e >> 1) * 4096);
        b1 = *lp<u32x4>(ac[e & 1] + slot + (e >> 1) * 4096 + 2048);
      }
      o0 = mfma(Hb[s % 3][+e], b0, o0);
      o1 = mfma(Hb[s % 3][+e], b1, o1);
    });
    __builtin_amdgcn_sched_barrier(0);
    if constexpr (s + 3 < 22) issue_h(std::integral_constant<int, s + 3>{});
  });
  ST(18);
  const u32 xb = (odd ? uslot(26) : uslot(28));
  {
    const f32x16 snd = kh ? o0 : o1;
    sfor<0, 4>([&](auto g) { *lp<f32x4>(xb + w * 4096 + g * 1024 + lane * 16) = f32x4{snd[4 * g], snd[4 * g + 1], snd[4 * g + 2], snd[4 * g + 3]}; });
  }
  asm volatile("s_waitcnt vmcnt(0) lgkmcnt(0)\n\ts_barrier"
               : "+v"(xe[0]), "+v"(xe[1]), "+v"(xe[2]), "+v"(xe[3]), "+v"(xe[4]), "+v"(xe[5]), "+v"(xe[6]), "+v"(xe[7]), "+v"(xe[8]),
                 "+v"(xe[9]), "+v"(xe[10]), "+v"(xe[11]), "+v"(xe[12]), "+v"(xe[13]), "+v"(xe[14]), "+v"(xe[15])::"memory");
  {
    const f32x16 own = kh ? o1 : o0;
    const rsrc_t ro = mkrs(pout, 2048 * 512 * 4);
    const float dsk = bf2f(qdsk), b2 = bf2f(qb2);
    const u32 vo = hh * 16 * 2048 + l31 * 4, so = (128 * r + 32 * (2 * ghalf + gm)) * 2048 + 256 * gc + 128 * kh;
    sfor<0, 4>([&](auto g) {
      const f32x4 p = *lp<f32x4>(xb + (w ^ 2) * 4096 + g * 1024 + lane * 16);
      const f32x4 mm = *lp<f32x4>(L_STM + (32 * (2 * ghalf + gm) + 16 * hh + 4 * g) * 4);
      const f32x4 rr = *lp<f32x4>(L_STR + (32 * (2 * ghalf + gm) + 16 * hh + 4 * g) * 4);
      sfor<0, 4>([&](auto e) {
        constexpr int t = 4 * g + e;
        const float xv = bf2f(xe[t]);
        const float v = (own[t] + p[+e]) + (xv + b2) + (xv - mm[+e]) * rr[+e] * dsk;
        __builtin_amdgcn_raw_buffer_store_b32(__float_as_uint(v), ro, vo, so + t * 2048, 0);
      });
    });
  }
  asm volatile(".Lcode_b:");
}

extern "C" void kernel_launch(void* const* d_in, const int* in_sizes, int n_in, void* d_out, int out_size, void* d_ws,
                              size_t ws_size, hipStream_t stream) {
  s5_mlp_fused<<<256, 256, 0, stream>>>((const u16*)d_in[0], (const u16*)d_in[1], (const u16*)d_in[2], (const u16*)d_in[3],
                                        (const u16*)d_in[4], (const u16*)d_in[5], (const u16*)d_in[6], (const u16*)d_in[7],
                                        (const u16*)d_in[8], (const u16*)d_in[10], (const u16*)d_in[11], (const u16*)d_in[12],
                                        (const u16*)d_in[13], (float*)d_out, (char*)d_ws);
}
```

```cpp
#include <hip/hip_runtime.h>

typedef unsigned u32;
__global__ __launch_bounds__(256, 1) void s5_mlp_fused(
    const void* x, u32 xn, u32 xf, const void* bre, u32 bren, u32 bref, const void* bim, u32 bimn, u32 bimf,
    const void* w1, u32 w1n, u32 w1f, const void* w2, u32 w2n, u32 w2f, const void* cre, u32 cren, u32 cref,
    const void* cim, u32 cimn, u32 cimf, void* ws, u32 wsn, u32 wsf, void* out, u32 outn, u32 outf,
    const void* b1, u32 b1n, u32 b1f, const void* lre, const void* lim, const void* ldt, const void* dsk, const void* b2) {
  __shared__ __attribute__((aligned(1024))) char smem[163840];
  asm volatile(
    "s_load_dwordx16 s[4:19], s[0:1], 0\n"
    "s_load_dwordx16 s[20:35], s[0:1], 64\n"
    "s_load_dwordx16 s[36:51], s[0:1], 128\n"
    "s_load_dwordx2 s[52:53], s[0:1], 192\n"
    "s_getpc_b64 s[56:57]\n"
    ".Lca:\n"
    "v_and_b32 v5, 63, v0\n"
    "v_lshrrev_b32 v1, 3, v0\n"
    "v_and_b32 v2, 7, v0\n"
    "v_lshlrev_b32 v1, 7, v1\n"
    "v_lshl_add_u32 v1, v2, 26, v1\n"
    "s_and_b32 s56, s56, 0xffffff80\n"
    "s_mov_b32 s58, .Lcb-.Lca\n"
    "s_mov_b32 s59, 0x20000\n"
    "s_bfe_u32 s68, s2, 0x30003\n"
    "s_lshl_b32 s68, s68, 12\n"
    "buffer_load_dword v6, v1, s[56:59], s68 offen\n"
    "v_readfirstlane_b32 s60, v0\n"
    "s_bfe_u32 s61, s2, 0x10002\n"
    "s_lshr_b32 s65, s2, 5\n"
    "s_lshl3_add_u32 s61, s61, s65\n"
    "s_bfe_u32 s68, s2, 0x20003\n"
    "s_and_b32 s69, s2, 3\n"
    "s_lshl2_add_u32 s62, s69, s68\n"
    "s_lshr_b32 s60, s60, 6\n"
    "v_lshrrev_b32 v1, 1, v5\n"
    "v_bfe_u32 v2, v5, 3, 2\n"
    "v_and_or_b32 v1, v1, 16, v2\n"
    "v_lshrrev_b32 v2, 4, v5\n"
    "v_and_b32 v3, 7, v5\n"
    "v_xor_b32 v2, v2, v3\n"
    "v_lshlrev_b32 v87, 4, v2\n"
    "v_xor_b32 v88, 64, v87\n"
    "v_lshrrev_b32 v86, 3, v5\n"
    "v_lshl_add_u32 v80, v1, 10, v87\n"
    "v_lshl_add_u32 v81, v1, 10, v88\n"
    "v_lshl_add_u32 v84, v86, 10, v87\n"
    "v_lshl_add_u32 v85, v86, 10, v88\n"
    "v_add_u32 v81, 3072, v81\n"
    "v_add_u32 v85, 7168, v85\n"
    "v_add_u32 v82, 6144, v80\n"
    "v_add_u32 v83, 6144, v81\n"
    "s_lshl_b32 s66, s61, 17\n"
    "s_lshl_b32 s68, s60, 15\n"
    "s_add_u32 s66, s66, s68\n"
    "s_lshl_b32 s67, s62, 15\n"
    "s_and_b32 s68, s60, 1\n"
    "s_lshl_b32 s68, s68, 14\n"
    "s_add_u32 s67, s67, s68\n"
    "s_lshl_b32 s72, s60, 12\n"
    "s_lshl_b32 s73, s60, 11\n"
    "s_addk_i32 s73, 16384\n"
    "s_and_b32 s68, s62, 3\n"
    "s_lshl1_add_u32 s75, s68, s65\n"
    "s_lshl_b32 s75, s75, 7\n"
    "s_cmp_lt_u32 s60, 2\n"
    "s_waitcnt lgkmcnt(0)\n"
    "s_cselect_b64 s[8:9], s[8:9], s[12:13]\n"
    "s_add_u32 s76, s75, 0\n"
    "s_and_b32 s76, s76, 1023\n"
    "s_add_u32 m0, s72, 0\n"
    "s_add_u32 s74, s66, s76\n"
    "buffer_load_dwordx4 v80, s[4:7], s74 offen offset:0 lds\n"
    "buffer_load_dwordx4 v81, s[4:7], s74 offen offset:1024 lds\n"
    "buffer_load_dwordx4 v82, s[4:7], s74 offen offset:2048 lds\n"
    "buffer_load_dwordx4 v83, s[4:7], s74 offen offset:3072 lds\n"
    "s_add_u32 m0, s73, 0\n"
    "s_add_u32 s74, s67, s76\n"
    "buffer_load_dwordx4 v84, s[8:11], s74 offen offset:0 lds\n"
    "buffer_load_dwordx4 v85, s[8:11], s74 offen offset:1024 lds\n"
    "s_add_u32 s76, s75, 128\n"
    "s_and_b32 s76, s76, 1023\n"
    "s_add_u32 m0, s72, 24576\n"
    "s_add_u32 s74, s66, s76\n"
    "buffer_load_dwordx4 v80, s[4:7], s74 offen offset:0 lds\n"
    "buffer_load_dwordx4 v81, s[4:7], s74 offen offset:1024 lds\n"
    "buffer_load_dwordx4 v82, s[4:7], s74 offen offset:2048 lds\n"
    "buffer_load_dwordx4 v83, s[4:7], s74 offen offset:3072 lds\n"
    "s_add_u32 m0, s73, 24576\n"
    "s_add_u32 s74, s67, s76\n"
    "buffer_load_dwordx4 v84, s[8:11], s74 offen offset:0 lds\n"
    "buffer_load_dwordx4 v85, s[8:11], s74 offen offset:1024 lds\n"
    "v_and_b32 v7, 31, v5\n"
    "v_lshrrev_b32 v8, 5, v5\n"
    "v_bfe_u32 v1, v5, 1, 3\n"
    "v_xor_b32 v1, v1, v8\n"
    "v_lshlrev_b32 v2, 7, v7\n"
    "s_add_u32 s68, s60, 0\n"
    "s_and_b32 s68, s68, 3\n"
    "s_lshl_b32 s68, s68, 1\n"
    "v_xor_b32 v3, s68, v1\n"
    "v_lshl_add_u32 v3, v3, 4, v2\n"
    "v_add_u32 v93, s72, v3\n"
    "v_add_u32 v89, 16384, v3\n"
    "s_add_u32 s68, s60, 1\n"
    "s_and_b32 s68, s68, 3\n"
    "s_lshl_b32 s68, s68, 1\n"
    "v_xor_b32 v3, s68, v1\n"
    "v_lshl_add_u32 v3, v3, 4, v2\n"
    "v_add_u32 v94, s72, v3\n"
    "v_add_u32 v90, 16384, v3\n"
    "s_add_u32 s68, s60, 2\n"
    "s_and_b32 s68, s68, 3\n"
    "s_lshl_b32 s68, s68, 1\n"
    "v_xor_b32 v3, s68, v1\n"
    "v_lshl_add_u32 v3, v3, 4, v2\n"
    "v_add_u32 v95, s72, v3\n"
    "v_add_u32 v91, 16384, v3\n"
    "s_add_u32 s68, s60, 3\n"
    "s_and_b32 s68, s68, 3\n"
    "s_lshl_b32 s68, s68, 1\n"
    "v_xor_b32 v3, s68, v1\n"
    "v_lshl_add_u32 v3, v3, 4, v2\n"
    "v_add_u32 v224, s72, v3\n"
    "v_add_u32 v92, 16384, v3\n"
    "s_mov_b32 s77, 0x3f803f80\n"
    "v_mov_b32 v225, 0\n"
    "v_mov_b32 v226, 0\n"
    "v_mov_b32 v227, 0\n"
    "v_mov_b32 v228, 0\n"
    "v_mov_b32 v229, 0\n"
    "v_mov_b32 v230, 0\n"
    "s_and_b32 s63, s62, 3\n"
    "s_lshr_b32 s64, s62, 2\n"
    "s_mul_i32 s78, s64, 14\n"
    "s_lshl2_add_u32 s78, s63, s78\n"
    "s_lshl_b32 s68, s62, 6\n"
    "v_lshl_add_u32 v1, v7, 1, s68\n"
    "global_load_ushort v231, v1, s[44:45]\n"
    "global_load_ushort v232, v1, s[46:47]\n"
    "global_load_ushort v233, v1, s[48:49]\n"
    "v_mov_b32 v3, 0\n"
    "s_lshl_b32 s68, s61, 7\n"
    "s_add_u32 s68, s68, 28835840\n"
    "buffer_load_dwordx2 v[234:235], v3, s[32:35], s68 offen sc1\n"
    "s_lshl_b32 s68, s62, 7\n"
    "s_add_u32 s68, s68, 28837888\n"
    "buffer_load_dwordx2 v[236:237], v3, s[32:35], s68 offen sc1\n"
    "v_lshlrev_b32 v2, 29, v8\n"
    "v_lshl_add_u32 v2, v7, 1, v2\n"
    "s_lshl_b32 s68, s78, 6\n"
    "buffer_load_ushort v11, v2, s[40:43], s68 offen offset:0\n"
    "buffer_load_ushort v12, v2, s[40:43], s68 offen offset:64\n"
    "buffer_load_ushort v13, v2, s[40:43], s68 offen offset:128\n"
    "buffer_load_ushort v14, v2, s[40:43], s68 offen offset:192\n"
    "s_lshl2_add_u32 s68, s63, s64\n"
    "s_lshl_b32 s68, s68, 6\n"
    "v_lshl_add_u32 v1, v7, 1, s68\n"
    "global_load_ushort v9, v1, s[50:51]\n"
    "global_load_ushort v10, v1, s[52:53]\n"
    "v_and_b32 v1, 2, v86\n"
    "v_lshlrev_b32 v1, 1, v1\n"
    "v_and_b32 v2, 7, v5\n"
    "v_xor_b32 v1, v1, v2\n"
    "v_lshlrev_b32 v1, 4, v1\n"
    "v_mul_u32_u24 v2, 3584, v86\n"
    "v_add_u32 v15, v1, v2\n"
    "v_lshl_add_u32 v19, v86, 10, v1\n"
    "v_add_u32 v16, 27648, v15\n"
    "v_add_u32 v20, 7168, v19\n"
    "v_add_u32 v17, 55296, v15\n"
    "v_add_u32 v21, 14336, v19\n"
    "v_add_u32 v18, 82944, v15\n"
    "v_add_u32 v22, 21504, v19\n"
    "s_lshr_b32 s68, s75, 7\n"
    "s_add_u32 s68, s68, s60\n"
    "s_add_u32 s68, s68, 0\n"
    "s_and_b32 s68, s68, 7\n"
    "s_mul_i32 s68, s68, 229376\n"
    "s_lshl_b32 s69, s78, 6\n"
    "s_add_u32 s12, s68, s69\n"
    "s_add_u32 s68, s12, 128\n"
    "s_cmp_eq_u32 s63, 3\n"
    "s_cselect_b32 s14, 1072693248, s68\n"
    "s_lshr_b32 s68, s75, 7\n"
    "s_add_u32 s68, s68, s60\n"
    "s_add_u32 s68, s68, 4\n"
    "s_and_b32 s68, s68, 7\n"
    "s_mul_i32 s68, s68, 229376\n"
    "s_lshl_b32 s69, s78, 6\n"
    "s_add_u32 s13, s68, s69\n"
    "s_add_u32 s68, s13, 128\n"
    "s_cmp_eq_u32 s63, 3\n"
    "s_cselect_b32 s15, 1072693248, s68\n"
    "s_lshl_b32 s40, s60, 13\n"
    "v_lshrrev_b32 v1, 1, v7\n"
    "v_and_b32 v2, 1, v5\n"
    "v_lshlrev_b32 v2, 7, v2\n"
    "v_lshl_add_u32 v2, v8, 29, v2\n"
    "v_lshl_add_u32 v239, v1, 10, v2\n"
    "v_mul_u32_u24 v1, 3584, v1\n"
    "v_add_u32 v238, v1, v2\n"
    "s_lshl2_add_u32 s70, s65, s60\n"
    "s_mul_i32 s68, s70, 57344\n"
    "s_lshl_b32 s69, s78, 6\n"
    "s_add_u32 s81, s68, s69\n"
    "s_mul_i32 s68, s70, 14\n"
    "s_mul_i32 s69, s64, 448\n"
    "s_add_u32 s68, s68, s69\n"
    "s_lshl2_add_u32 s68, s68, s63\n"
    "s_lshl_b32 s82, s68, 8\n"
    "s_lshl3_add_u32 s68, s63, s65\n"
    "s_lshl_b32 s68, s68, 14\n"
    "s_lshl_b32 s69, s64, 8\n"
    "s_add_u32 s68, s68, s69\n"
    "s_bitcmp1_b32 s60, 0\n"
    "s_cselect_b32 s83, 1072693248, s68\n"
    "s_cmp_lt_u32 s60, 2\n"
    "s_cselect_b64 s[24:25], s[24:25], s[28:29]\n"
    "buffer_load_dword v6, v238, s[16:19], s81 offen\n"
    "v_bfe_u32 v2, v5, 3, 1\n"
    "v_lshlrev_b32 v2, 6, v2\n"
    "v_bfe_u32 v3, v5, 4, 1\n"
    "v_lshl_add_u32 v2, v3, 5, v2\n"
    "v_and_b32 v3, 3, v5\n"
    "v_lshl_add_u32 v2, v3, 3, v2\n"
    "v_bfe_u32 v3, v5, 2, 2\n"
    "v_lshl_add_u32 v41, v3, 7, v2\n"
    "v_lshl_add_u32 v2, v8, 10, v41\n"
    "s_add_u32 s68, s60, 0\n"
    "s_and_b32 s68, s68, 3\n"
    "s_lshl_b32 s68, s68, 11\n"
    "s_add_u32 s68, s68, 81920\n"
    "v_add_u32 v27, s68, v2\n"
    "s_add_u32 s68, s60, 1\n"
    "s_and_b32 s68, s68, 3\n"
    "s_lshl_b32 s68, s68, 11\n"
    "s_add_u32 s68, s68, 81920\n"
    "v_add_u32 v38, s68, v2\n"
    "s_add_u32 s68, s60, 2\n"
    "s_and_b32 s68, s68, 3\n"
    "s_lshl_b32 s68, s68, 11\n"
    "s_add_u32 s68, s68, 81920\n"
    "v_add_u32 v39, s68, v2\n"
    "s_add_u32 s68, s60, 3\n"
    "s_and_b32 s68, s68, 3\n"
    "s_lshl_b32 s68, s68, 11\n"
    "s_add_u32 s68, s68, 81920\n"
    "v_add_u32 v40, s68, v2\n"
    "s_mov_b32 s29, 0x07060302\n"
    "s_mov_b32 s30, 0x05040302\n"
    "s_mov_b32 s90, 0\n"
    "s_movk_i32 s87, 8192\n"
    "v_lshlrev_b32 v52, 2, v5\n"
    "v_lshlrev_b32 v42, 4, v5\n"
    "v_add_u32 v43, 147456, v42\n"
    "s_lshl_b32 s68, s60, 2\n"
    "v_add_u32 v45, s68, v43\n"
    "v_lshl_add_u32 v214, v8, 1, s68\n"
    "v_subrev_u32 v214, s61, v214\n"
    "s_lshl_b32 s68, s60, 7\n"
    "s_add_u32 s68, s68, 154624\n"
    "v_lshl_add_u32 v44, v8, 6, s68\n"
    "v_lshlrev_b32 v3, 3, v7\n"
    "v_add_u32 v46, 152576, v3\n"
    "s_lshl_b32 s68, s60, 9\n"
    "v_lshl_add_u32 v47, v8, 8, v46\n"
    "v_add_u32 v47, s68, v47\n"
    "v_add_u32 v3, 147456, v3\n"
    "v_add_u32 v1, 15, v214\n"
    "v_and_b32 v1, 15, v1\n"
    "v_lshl_add_u32 v212, v1, 8, v3\n"
    "v_and_b32 v1, 15, v214\n"
    "v_lshl_add_u32 v213, v1, 8, v3\n"
    "s_lshl2_add_u32 s28, s61, s60\n"
    "s_mul_i32 s28, s28, 180224\n"
    "s_lshl2_add_u32 s31, s61, s64\n"
    "s_lshl2_add_u32 s31, s31, s60\n"
    "s_lshl_b32 s31, s31, 9\n"
    "s_add_u32 s31, s31, 28704768\n"
    "s_add_u32 s68, s63, 1\n"
    "v_add_u32 v48, s68, v5\n"
    "v_and_b32 v48, 3, v48\n"
    "v_lshlrev_b32 v48, 7, v48\n"
    "v_add_u32 v1, 1, v5\n"
    "v_lshrrev_b32 v1, 2, v1\n"
    "v_lshl_add_u32 v48, v1, 25, v48\n"
    "s_lshl_b32 s68, s63, 7\n"
    "v_lshrrev_b32 v1, 4, v5\n"
    "v_lshlrev_b32 v1, 26, v1\n"
    "v_lshl_add_u32 v1, v5, 3, v1\n"
    "v_add_u32 v49, s68, v1\n"
    "v_lshlrev_b32 v50, 4, v7\n"
    "s_lshl_b32 s69, s62, 9\n"
    "v_lshl_add_u32 v51, v8, 14, v50\n"
    "v_lshl_add_u32 v50, v8, 3, v50\n"
    "s_cmp_eq_u32 s60, 0\n"
    "s_cselect_b32 s68, 0, 1072693248\n"
    "v_add_u32 v50, s68, v50\n"
    "s_lshl_b32 s68, s60, 15\n"
    "s_add_u32 s68, s68, s69\n"
    "s_add_u32 s68, s68, 28573696\n"
    "v_add_u32 v51, s68, v51\n"
    "s_lshl_b32 s88, s61, 13\n"
    "s_add_u32 s88, s88, s69\n"
    "s_add_u32 s88, s88, 28573696\n"
    "s_lshl_b32 s89, s62, 12\n"
    "s_add_u32 s89, s89, s28\n"
    "s_add_u32 s89, s89, 114688\n"
    "s_cmp_eq_u32 s63, 3\n"
    "s_cselect_b32 s70, 0, s63\n"
    "s_add_u32 s68, s63, 0\n"
    "s_and_b32 s68, s68, 3\n"
    "s_lshl_b32 s41, s68, 2\n"
    "s_add_u32 s68, s63, 1\n"
    "s_and_b32 s68, s68, 3\n"
    "s_lshl_b32 s42, s68, 2\n"
    "s_add_u32 s68, s63, 2\n"
    "s_and_b32 s68, s68, 3\n"
    "s_lshl_b32 s43, s68, 2\n"
    "s_add_u32 s68, s63, 3\n"
    "s_and_b32 s68, s68, 3\n"
    "s_lshl_b32 s44, s68, 2\n"
    "s_add_u32 s68, s70, 0\n"
    "s_cmp_ge_u32 s68, 3\n"
    "s_cselect_b32 s69, 3, 0\n"
    "s_sub_u32 s68, s68, s69\n"
    "s_lshl2_add_u32 s45, s68, 2\n"
    "s_add_u32 s68, s70, 1\n"
    "s_cmp_ge_u32 s68, 3\n"
    "s_cselect_b32 s69, 3, 0\n"
    "s_sub_u32 s68, s68, s69\n"
    "s_lshl2_add_u32 s46, s68, 2\n"
    "s_add_u32 s68, s70, 2\n"
    "s_cmp_ge_u32 s68, 3\n"
    "s_cselect_b32 s69, 3, 0\n"
    "s_sub_u32 s68, s68, s69\n"
    "s_lshl2_add_u32 s47, s68, 2\n"
    "s_lshr_b32 s68, s64, 1\n"
    "s_lshl1_add_u32 s68, s63, s68\n"
    "s_lshl_b32 s68, s68, 7\n"
    "s_mul_i32 s69, s64, 14\n"
    "s_and_b32 s70, s60, 1\n"
    "s_add_u32 s69, s69, s70\n"
    "s_lshl_b32 s69, s69, 15\n"
    "s_add_u32 s71, s68, s69\n"
    "s_xor_b32 s68, s68, 128\n"
    "s_add_u32 s70, s68, s69\n"
    "s_bitcmp1_b32 s60, 1\n"
    "s_cselect_b32 s68, s42, s41\n"
    "s_lshl_b32 s68, s68, 15\n"
    "s_add_u32 s48, s68, s70\n"
    "s_bitcmp1_b32 s60, 1\n"
    "s_cselect_b32 s68, s44, s43\n"
    "s_lshl_b32 s68, s68, 15\n"
    "s_add_u32 s49, s68, s70\n"
    "s_bitcmp1_b32 s60, 1\n"
    "s_cselect_b32 s68, s46, s45\n"
    "s_lshl_b32 s68, s68, 15\n"
    "s_add_u32 s50, s68, s70\n"
    "s_bitcmp1_b32 s60, 1\n"
    "s_cselect_b32 s68, s42, s41\n"
    "s_lshl_b32 s68, s68, 15\n"
    "s_add_u32 s51, s68, s71\n"
    "s_bitcmp1_b32 s60, 1\n"
    "s_cselect_b32 s68, s44, s43\n"
    "s_lshl_b32 s68, s68, 15\n"
    "s_add_u32 s52, s68, s71\n"
    "s_bitcmp1_b32 s60, 1\n"
    "s_cselect_b32 s68, s45, s47\n"
    "s_cselect_b32 s69, s71, s70\n"
    "s_lshl_b32 s68, s68, 15\n"
    "s_add_u32 s53, s68, s69\n"
    "s_bitcmp1_b32 s60, 1\n"
    "s_cselect_b32 s68, s47, s46\n"
    "s_lshl_b32 s68, s68, 15\n"
    "s_add_u32 s79, s68, s71\n"
    "v_mov_b32 v23, v84\n"
    "v_mov_b32 v24, v85\n"
    "v_add_u32 v25, 14336, v84\n"
    "v_add_u32 v26, 14336, v85\n"
    "s_lshl_b32 s80, s63, 17\n"
    "s_and_b32 s68, s60, 1\n"
    "s_lshl1_add_u32 s68, s64, s68\n"
    "s_lshl_b32 s68, s68, 7\n"
    "s_add_u32 s80, s80, s68\n"
    "s_lshl_b32 s68, s60, 11\n"
    "s_add_u32 s68, s68, 155648\n"
    "v_bfe_u32 v1, v5, 1, 2\n"
    "v_lshlrev_b32 v2, 1, v8\n"
    "v_xor_b32 v1, v1, v2\n"
    "v_lshlrev_b32 v1, 4, v1\n"
    "v_lshl_add_u32 v1, v7, 6, v1\n"
    "v_add_u32 v208, s68, v1\n"
    "v_xor_b32 v209, 16, v208\n"
    "v_bfe_u32 v1, v5, 2, 2\n"
    "v_lshl_add_u32 v2, v8, 3, v1\n"
    "v_lshlrev_b32 v2, 6, v2\n"
    "v_lshrrev_b32 v1, 1, v1\n"
    "v_and_b32 v3, 1, v5\n"
    "v_lshlrev_b32 v3, 1, v3\n"
    "v_xor_b32 v1, v1, v3\n"
    "v_bfe_u32 v3, v5, 4, 1\n"
    "v_xor_b32 v1, v1, v3\n"
    "v_lshl_add_u32 v2, v1, 4, v2\n"
    "v_bfe_u32 v1, v5, 1, 1\n"
    "v_lshl_add_u32 v2, v1, 3, v2\n"
    "v_add_u32 v210, s68, v2\n"
    "v_xor_b32 v211, 32, v210\n"
    "s_lshl_b32 s71, s64, 14\n"
    "s_add_u32 s71, s71, s28\n"
    "s_add_u32 s71, s71, 114688\n"
    "s_lshl2_add_u32 s69, s61, s60\n"
    "s_lshl_b32 s69, s69, 12\n"
    "s_add_u32 s69, s69, 28311552\n"
    "s_lshl_b32 s97, s62, 8\n"
    "s_add_u32 s97, s97, s69\n"
    "s_lshl_b32 s68, s64, 10\n"
    "s_add_u32 s69, s69, s68\n"
    "s_sub_u32 s69, s69, 128\n"
    "s_lshl_b32 s68, s42, 10\n"
    "s_add_u32 s91, s68, s71\n"
    "s_lshl_b32 s68, s45, 6\n"
    "s_add_u32 s94, s68, s69\n"
    "s_lshl_b32 s68, s43, 10\n"
    "s_add_u32 s92, s68, s71\n"
    "s_lshl_b32 s68, s46, 6\n"
    "s_add_u32 s95, s68, s69\n"
    "s_lshl_b32 s68, s44, 10\n"
    "s_add_u32 s93, s68, s71\n"
    "s_lshl_b32 s68, s47, 6\n"
    "s_add_u32 s96, s68, s69\n"
    "s_mul_i32 s70, s64, 28672\n"
    "s_add_u32 s70, s70, s28\n"
    "s_lshl_b32 s42, s42, 11\n"
    "s_add_u32 s42, s42, s70\n"
    "s_lshl_b32 s43, s43, 11\n"
    "s_add_u32 s43, s43, s70\n"
    "s_lshl_b32 s44, s44, 11\n"
    "s_add_u32 s44, s44, s70\n"
    "s_lshl_b32 s45, s45, 11\n"
    "s_add_u32 s45, s45, s70\n"
    "s_lshl_b32 s46, s46, 11\n"
    "s_add_u32 s46, s46, s70\n"
    "s_lshl_b32 s47, s47, 11\n"
    "s_add_u32 s47, s47, s70\n"
    "s_waitcnt vmcnt(18)\n"
    "s_barrier\n"
    "ds_read_b128 v[192:195], v93 offset:0\n"
    "ds_read_b128 v[196:199], v94 offset:0\n"
    "ds_read_b128 v[200:203], v95 offset:0\n"
    "ds_read_b128 v[204:207], v224 offset:0\n"
    "ds_read_b128 v[128:131], v89 offset:0\n"
    "ds_read_b128 v[144:147], v89 offset:4096\n"
    "ds_read_b128 a[0:3], v93 offset:0\n"
    "ds_read_b128 a[4:7], v94 offset:0\n"
    "ds_read_b128 a[8:11], v95 offset:0\n"
    "ds_read_b128 a[12:15], v224 offset:0\n"
    "ds_read_b128 v[132:135], v90 offset:0\n"
    "ds_read_b128 v[148:151], v90 offset:4096\n"
    "ds_read_b128 v[136:139], v91 offset:0\n"
    "ds_read_b128 v[152:155], v91 offset:4096\n"
    "ds_read_b128 v[140:143], v92 offset:0\n"
    "ds_read_b128 v[156:159], v92 offset:4096\n"
    "s_add_u32 s76, s75, 256\n"
    "s_and_b32 s76, s76, 1023\n"
    "s_add_u32 m0, s72, 49152\n"
    "s_add_u32 s74, s66, s76\n"
    "buffer_load_dwordx4 v80, s[4:7], s74 offen offset:0 lds\n"
    "buffer_load_dwordx4 v81, s[4:7], s74 offen offset:1024 lds\n"
    "buffer_load_dwordx4 v82, s[4:7], s74 offen offset:2048 lds\n"
    "buffer_load_dwordx4 v83, s[4:7], s74 offen offset:3072 lds\n"
    "s_add_u32 m0, s73, 49152\n"
    "s_add_u32 s74, s67, s76\n"
    "buffer_load_dwordx4 v84, s[8:11], s74 offen offset:0 lds\n"
    "buffer_load_dwordx4 v85, s[8:11], s74 offen offset:1024 lds\n"
    "s_waitcnt lgkmcnt(10)\n"
    "v_dot2c_f32_bf16 v225, s77, v192\n"
    "v_dot2c_f32_bf16 v227, v192, v192\n"
    "v_dot2c_f32_bf16 v226, s77, v193\n"
    "v_dot2c_f32_bf16 v228, v193, v193\n"
    "v_dot2c_f32_bf16 v229, s77, v128\n"
    "v_dot2c_f32_bf16 v225, s77, v194\n"
    "v_dot2c_f32_bf16 v227, v194, v194\n"
    "v_dot2c_f32_bf16 v226, s77, v195\n"
    "v_dot2c_f32_bf16 v228, v195, v195\n"
    "v_dot2c_f32_bf16 v230, s77, v144\n"
    "v_dot2c_f32_bf16 v225, s77, v196\n"
    "v_dot2c_f32_bf16 v227, v196, v196\n"
    "v_dot2c_f32_bf16 v226, s77, v197\n"
    "v_dot2c_f32_bf16 v228, v197, v197\n"
    "v_dot2c_f32_bf16 v229, s77, v129\n"
    "v_dot2c_f32_bf16 v225, s77, v198\n"
    "v_dot2c_f32_bf16 v227, v198, v198\n"
    "v_dot2c_f32_bf16 v226, s77, v199\n"
    "v_dot2c_f32_bf16 v228, v199, v199\n"
    "v_dot2c_f32_bf16 v230, s77, v145\n"
    "v_dot2c_f32_bf16 v225, s77, v200\n"
    "v_dot2c_f32_bf16 v227, v200, v200\n"
    "v_dot2c_f32_bf16 v226, s77, v201\n"
    "v_dot2c_f32_bf16 v228, v201, v201\n"
    "v_dot2c_f32_bf16 v229, s77, v130\n"
    "v_dot2c_f32_bf16 v225, s77, v202\n"
    "v_dot2c_f32_bf16 v227, v202, v202\n"
    "v_dot2c_f32_bf16 v226, s77, v203\n"
    "v_dot2c_f32_bf16 v228, v203, v203\n"
    "v_dot2c_f32_bf16 v230, s77, v146\n"
    "v_dot2c_f32_bf16 v225, s77, v204\n"
    "v_dot2c_f32_bf16 v227, v204, v204\n"
    "v_dot2c_f32_bf16 v226, s77, v205\n"
    "v_dot2c_f32_bf16 v228, v205, v205\n"
    "v_dot2c_f32_bf16 v229, s77, v131\n"
    "v_dot2c_f32_bf16 v225, s77, v206\n"
    "v_dot2c_f32_bf16 v227, v206, v206\n"
    "v_dot2c_f32_bf16 v226, s77, v207\n"
    "v_dot2c_f32_bf16 v228, v207, v207\n"
    "v_dot2c_f32_bf16 v230, s77, v147\n"
    "s_waitcnt vmcnt(18) lgkmcnt(0)\n"
    "s_barrier\n"
    "v_mfma_f32_32x32x16_bf16 v[96:111], a[0:3], v[128:131], 0\n"
    "ds_read_b128 v[192:195], v93 offset:24576\n"
    "ds_read_b128 v[196:199], v94 offset:24576\n"
    "s_add_u32 s76, s75, 384\n"
    "s_and_b32 s76, s76, 1023\n"
    "s_add_u32 m0, s72, 0\n"
    "v_mfma_f32_32x32x16_bf16 v[112:127], a[0:3], v[144:147], 0\n"
    "ds_read_b128 v[200:203], v95 offset:24576\n"
    "ds_read_b128 v[204:207], v224 offset:24576\n"
    "s_add_u32 s74, s66, s76\n"
    "buffer_load_dwordx4 v80, s[4:7], s74 offen offset:0 lds\n"
    "v_mfma_f32_32x32x16_bf16 v[96:111], a[4:7], v[132:135], v[96:111]\n"
    "ds_read_b128 v[160:163], v89 offset:24576\n"
    "ds_read_b128 v[176:179], v89 offset:28672\n"
    "buffer_load_dwordx4 v81, s[4:7], s74 offen offset:1024 lds\n"
    "v_mfma_f32_32x32x16_bf16 v[112:127], a[4:7], v[148:151], v[112:127]\n"
    "ds_read_b128 a[16:19], v93 offset:24576\n"
    "ds_read_b128 a[20:23], v94 offset:24576\n"
    "buffer_load_dwordx4 v82, s[4:7], s74 offen offset:2048 lds\n"
    "v_mfma_f32_32x32x16_bf16 v[96:111], a[8:11], v[136:139], v[96:111]\n"
    "ds_read_b128 a[24:27], v95 offset:24576\n"
    "ds_read_b128 a[28:31], v224 offset:24576\n"
    "buffer_load_dwordx4 v83, s[4:7], s74 offen offset:3072 lds\n"
    "s_add_u32 m0, s73, 0\n"
    "s_add_u32 s74, s67, s76\n"
    "v_mfma_f32_32x32x16_bf16 v[112:127], a[8:11], v[152:155], v[112:127]\n"
    "ds_read_b128 v[164:167], v90 offset:24576\n"
    "ds_read_b128 v[180:183], v90 offset:28672\n"
    "buffer_load_dwordx4 v84, s[8:11], s74 offen offset:0 lds\n"
    "v_mfma_f32_32x32x16_bf16 v[96:111], a[12:15], v[140:143], v[96:111]\n"
    "ds_read_b128 v[168:171], v91 offset:24576\n"
    "ds_read_b128 v[184:187], v91 offset:28672\n"
    "buffer_load_dwordx4 v85, s[8:11], s74 offen offset:1024 lds\n"
    "v_mfma_f32_32x32x16_bf16 v[112:127], a[12:15], v[156:159], v[112:127]\n"
    "ds_read_b128 v[172:175], v92 offset:24576\n"
    "ds_read_b128 v[188:191], v92 offset:28672\n"
    "s_waitcnt lgkmcnt(10)\n"
    "v_dot2c_f32_bf16 v225, s77, v192\n"
    "v_dot2c_f32_bf16 v227, v192, v192\n"
    "v_dot2c_f32_bf16 v226, s77, v193\n"
    "v_dot2c_f32_bf16 v228, v193, v193\n"
    "v_dot2c_f32_bf16 v229, s77, v160\n"
    "v_dot2c_f32_bf16 v225, s77, v194\n"
    "v_dot2c_f32_bf16 v227, v194, v194\n"
    "v_dot2c_f32_bf16 v226, s77, v195\n"
    "v_dot2c_f32_bf16 v228, v195, v195\n"
    "v_dot2c_f32_bf16 v230, s77, v176\n"
    "v_dot2c_f32_bf16 v225, s77, v196\n"
    "v_dot2c_f32_bf16 v227, v196, v196\n"
    "v_dot2c_f32_bf16 v226, s77, v197\n"
    "v_dot2c_f32_bf16 v228, v197, v197\n"
    "v_dot2c_f32_bf16 v229, s77, v161\n"
    "v_dot2c_f32_bf16 v225, s77, v198\n"
    "v_dot2c_f32_bf16 v227, v198, v198\n"
    "v_dot2c_f32_bf16 v226, s77, v199\n"
    "v_dot2c_f32_bf16 v228, v199, v199\n"
    "v_dot2c_f32_bf16 v230, s77, v177\n"
    "v_dot2c_f32_bf16 v225, s77, v200\n"
    "v_dot2c_f32_bf16 v227, v200, v200\n"
    "v_dot2c_f32_bf16 v226, s77, v201\n"
    "v_dot2c_f32_bf16 v228, v201, v201\n"
    "v_dot2c_f32_bf16 v229, s77, v162\n"
    "v_dot2c_f32_bf16 v225, s77, v202\n"
    "v_dot2c_f32_bf16 v227, v202, v202\n"
    "v_dot2c_f32_bf16 v226, s77, v203\n"
    "v_dot2c_f32_bf16 v228, v203, v203\n"
    "v_dot2c_f32_bf16 v230, s77, v178\n"
    "v_dot2c_f32_bf16 v225, s77, v204\n"
    "v_dot2c_f32_bf16 v227, v204, v204\n"
    "v_dot2c_f32_bf16 v226, s77, v205\n"
    "v_dot2c_f32_bf16 v228, v205, v205\n"
    "v_dot2c_f32_bf16 v229, s77, v163\n"
    "v_dot2c_f32_bf16 v225, s77, v206\n"
    "v_dot2c_f32_bf16 v227, v206, v206\n"
    "v_dot2c_f32_bf16 v226, s77, v207\n"
    "v_dot2c_f32_bf16 v228, v207, v207\n"
    "v_dot2c_f32_bf16 v230, s77, v179\n"
    "s_waitcnt vmcnt(6) lgkmcnt(0)\n"
    "s_barrier\n"
    "v_mfma_f32_32x32x16_bf16 v[96:111], a[16:19], v[160:163], v[96:111]\n"
    "ds_read_b128 v[192:195], v93 offset:49152\n"
    "ds_read_b128 v[196:199], v94 offset:49152\n"
    "s_add_u32 s76, s75, 512\n"
    "s_and_b32 s76, s76, 1023\n"
    "s_add_u32 m0, s72, 24576\n"
    "v_lshlrev_b32 v242, 16, v233\n"
    "v_mfma_f32_32x32x16_bf16 v[112:127], a[16:19], v[176:179], v[112:127]\n"
    "ds_read_b128 v[200:203], v95 offset:49152\n"
    "ds_read_b128 v[204:207], v224 offset:49152\n"
    "s_add_u32 s74, s66, s76\n"
    "buffer_load_dwordx4 v80, s[4:7], s74 offen offset:0 lds\n"
    "v_lshlrev_b32 v240, 16, v231\n"
    "v_mul_f32 v242, 0x3fb8aa3b, v242\n"
    "v_mfma_f32_32x32x16_bf16 v[96:111], a[20:23], v[164:167], v[96:111]\n"
    "ds_read_b128 v[128:131], v89 offset:49152\n"
    "ds_read_b128 v[144:147], v89 offset:53248\n"
    "buffer_load_dwordx4 v81, s[4:7], s74 offen offset:1024 lds\n"
    "v_lshlrev_b32 v241, 16, v232\n"
    "v_exp_f32 v242, v242\n"
    "v_mul_f32 v244, v240, v240\n"
    "v_mfma_f32_32x32x16_bf16 v[112:127], a[20:23], v[180:183], v[112:127]\n"
    "ds_read_b128 a[32:35], v93 offset:49152\n"
    "ds_read_b128 a[36:39], v94 offset:49152\n"
    "buffer_load_dwordx4 v82, s[4:7], s74 offen offset:2048 lds\n"
    "v_cvt_pk_bf16_f32 v242, v242, v242\n"
    "v_fmac_f32 v244, v241, v241\n"
    "v_lshlrev_b32 v242, 16, v242\n"
    "v_mfma_f32_32x32x16_bf16 v[96:111], a[24:27], v[168:171], v[96:111]\n"
    "ds_read_b128 a[40:43], v95 offset:49152\n"
    "ds_read_b128 a[44:47], v224 offset:49152\n"
    "buffer_load_dwordx4 v83, s[4:7], s74 offen offset:3072 lds\n"
    "s_add_u32 m0, s73, 24576\n"
    "s_add_u32 s74, s67, s76\n"
    "v_rcp_f32 v31, v244\n"
    "v_mfma_f32_32x32x16_bf16 v[112:127], a[24:27], v[184:187], v[112:127]\n"
    "ds_read_b128 v[132:135], v90 offset:49152\n"
    "ds_read_b128 v[148:151], v90 offset:53248\n"
    "buffer_load_dwordx4 v84, s[8:11], s74 offen offset:0 lds\n"
    "v_mul_f32 v248, v242, v240\n"
    "v_mul_f32 v243, v242, v241\n"
    "v_mul_f32 v248, 0x3fb8aa3b, v248\n"
    "v_mfma_f32_32x32x16_bf16 v[96:111], a[28:31], v[172:175], v[96:111]\n"
    "ds_read_b128 v[136:139], v91 offset:49152\n"
    "ds_read_b128 v[152:155], v91 offset:53248\n"
    "buffer_load_dwordx4 v85, s[8:11], s74 offen offset:1024 lds\n"
    "v_mul_f32 v249, 0x3f22f983, v243\n"
    "v_exp_f32 v248, v248\n"
    "v_rndne_f32 v249, v249\n"
    "v_mfma_f32_32x32x16_bf16 v[112:127], a[28:31], v[188:191], v[112:127]\n"
    "ds_read_b128 v[140:143], v92 offset:49152\n"
    "ds_read_b128 v[156:159], v92 offset:53248\n"
    "v_add_f32 v242, v31, v31\n"
    "v_fmamk_f32 v244, v249, 0xbfc90fdb, v243\n"
    "v_fmac_f32 v244, 0x333bbd2e, v249\n"
    "v_cvt_i32_f32 v249, v249\n"
    "s_waitcnt lgkmcnt(10)\n"
    "v_dot2c_f32_bf16 v225, s77, v192\n"
    "v_dot2c_f32_bf16 v227, v192, v192\n"
    "v_dot2c_f32_bf16 v226, s77, v193\n"
    "v_dot2c_f32_bf16 v228, v193, v193\n"
    "v_dot2c_f32_bf16 v229, s77, v128\n"
    "v_dot2c_f32_bf16 v225, s77, v194\n"
    "v_dot2c_f32_bf16 v227, v194, v194\n"
    "v_dot2c_f32_bf16 v226, s77, v195\n"
    "v_dot2c_f32_bf16 v228, v195, v195\n"
    "v_dot2c_f32_bf16 v230, s77, v144\n"
    "v_dot2c_f32_bf16 v225, s77, v196\n"
    "v_dot2c_f32_bf16 v227, v196, v196\n"
    "v_dot2c_f32_bf16 v226, s77, v197\n"
    "v_dot2c_f32_bf16 v228, v197, v197\n"
    "v_dot2c_f32_bf16 v229, s77, v129\n"
    "v_dot2c_f32_bf16 v225, s77, v198\n"
    "v_dot2c_f32_bf16 v227, v198, v198\n"
    "v_dot2c_f32_bf16 v226, s77, v199\n"
    "v_dot2c_f32_bf16 v228, v199, v199\n"
    "v_dot2c_f32_bf16 v230, s77, v145\n"
    "v_dot2c_f32_bf16 v225, s77, v200\n"
    "v_dot2c_f32_bf16 v227, v200, v200\n"
    "v_dot2c_f32_bf16 v226, s77, v201\n"
    "v_dot2c_f32_bf16 v228, v201, v201\n"
    "v_dot2c_f32_bf16 v229, s77, v130\n"
    "v_dot2c_f32_bf16 v225, s77, v202\n"
    "v_dot2c_f32_bf16 v227, v202, v202\n"
    "v_dot2c_f32_bf16 v226, s77, v203\n"
    "v_dot2c_f32_bf16 v228, v203, v203\n"
    "v_dot2c_f32_bf16 v230, s77, v146\n"
    "v_dot2c_f32_bf16 v225, s77, v204\n"
    "v_dot2c_f32_bf16 v227, v204, v204\n"
    "v_dot2c_f32_bf16 v226, s77, v205\n"
    "v_dot2c_f32_bf16 v228, v205, v205\n"
    "v_dot2c_f32_bf16 v229, s77, v131\n"
    "v_dot2c_f32_bf16 v225, s77, v206\n"
    "v_dot2c_f32_bf16 v227, v206, v206\n"
    "v_dot2c_f32_bf16 v226, s77, v207\n"
    "v_dot2c_f32_bf16 v228, v207, v207\n"
    "v_dot2c_f32_bf16 v230, s77, v147\n"
    "s_waitcnt vmcnt(6) lgkmcnt(0)\n"
    "s_barrier\n"
    "v_mfma_f32_32x32x16_bf16 v[96:111], a[32:35], v[128:131], v[96:111]\n"
    "ds_read_b128 v[192:195], v93 offset:0\n"
    "ds_read_b128 v[196:199], v94 offset:0\n"
    "s_add_u32 s76, s75, 640\n"
    "s_and_b32 s76, s76, 1023\n"
    "s_add_u32 m0, s72, 49152\n"
    "v_mul_f32 v245, v244, v244\n"
    "v_mfma_f32_32x32x16_bf16 v[112:127], a[32:35], v[144:147], v[112:127]\n"
    "ds_read_b128 v[200:203], v95 offset:0\n"
    "ds_read_b128 v[204:207], v224 offset:0\n"
    "s_add_u32 s74, s66, s76\n"
    "buffer_load_dwordx4 v80, s[4:7], s74 offen offset:0 lds\n"
    "v_mov_b32 v246, 0x3638b88e\n"
    "v_mov_b32 v247, 0xb48bdd11\n"
    "v_mfma_f32_32x32x16_bf16 v[96:111], a[36:39], v[132:135], v[96:111]\n"
    "ds_read_b128 v[160:163], v89 offset:0\n"
    "ds_read_b128 v[176:179], v89 offset:4096\n"
    "buffer_load_dwordx4 v81, s[4:7], s74 offen offset:1024 lds\n"
    "v_fmaak_f32 v246, v246, v245, 0xb9500bf1\n"
    "v_fmaak_f32 v247, v247, v245, 0x37cfb4c2\n"
    "v_fmaak_f32 v246, v246, v245, 0x3c088886\n"
    "v_mfma_f32_32x32x16_bf16 v[112:127], a[36:39], v[148:151], v[112:127]\n"
    "ds_read_b128 a[48:51], v93 offset:0\n"
    "ds_read_b128 a[52:55], v94 offset:0\n"
    "buffer_load_dwordx4 v82, s[4:7], s74 offen offset:2048 lds\n"
    "v_fmaak_f32 v247, v247, v245, 0xbab609aa\n"
    "v_fmaak_f32 v246, v246, v245, 0xbe2aaaab\n"
    "v_fmaak_f32 v247, v247, v245, 0x3d2aaaa3\n"
    "v_mfma_f32_32x32x16_bf16 v[96:111], a[40:43], v[136:139], v[96:111]\n"
    "ds_read_b128 a[56:59], v95 offset:0\n"
    "ds_read_b128 a[60:63], v224 offset:0\n"
    "buffer_load_dwordx4 v83, s[4:7], s74 offen offset:3072 lds\n"
    "s_add_u32 m0, s73, 49152\n"
    "s_add_u32 s74, s67, s76\n"
    "v_mul_f32 v246, v246, v245\n"
    "v_mfma_f32_32x32x16_bf16 v[112:127], a[40:43], v[152:155], v[112:127]\n"
    "ds_read_b128 v[164:167], v90 offset:0\n"
    "ds_read_b128 v[180:183], v90 offset:4096\n"
    "buffer_load_dwordx4 v84, s[8:11], s74 offen offset:0 lds\n"
    "v_fma_f32 v247, v247, v245, -0.5\n"
    "v_fma_f32 v246, v246, v244, v244\n"
    "v_fma_f32 v247, v247, v245, 1.0\n"
    "v_mfma_f32_32x32x16_bf16 v[96:111], a[44:47], v[140:143], v[96:111]\n"
    "ds_read_b128 v[168:171], v91 offset:0\n"
    "ds_read_b128 v[184:187], v91 offset:4096\n"
    "buffer_load_dwordx4 v85, s[8:11], s74 offen offset:1024 lds\n"
    "v_and_b32 v244, 1, v249\n"
    "v_lshlrev_b32 v245, 30, v249\n"
    "v_cmp_eq_u32 vcc, 1, v244\n"
    "v_add_u32 v249, 1, v249\n"
    "v_and_b32 v245, 0x80000000, v245\n"
    "v_cndmask_b32 v29, v246, v247, vcc\n"
    "v_cndmask_b32 v28, v247, v246, vcc\n"
    "v_mfma_f32_32x32x16_bf16 v[112:127], a[44:47], v[156:159], v[112:127]\n"
    "ds_read_b128 v[172:175], v92 offset:0\n"
    "ds_read_b128 v[188:191], v92 offset:4096\n"
    "v_lshlrev_b32 v249, 30, v249\n"
    "v_xor_b32 v29, v29, v245\n"
    "v_and_b32 v249, 0x80000000, v249\n"
    "v_mul_f32 v29, v248, v29\n"
    "s_waitcnt lgkmcnt(10)\n"
    "v_dot2c_f32_bf16 v225, s77, v192\n"
    "v_dot2c_f32_bf16 v227, v192, v192\n"
    "v_dot2c_f32_bf16 v226, s77, v193\n"
    "v_dot2c_f32_bf16 v228, v193, v193\n"
    "v_dot2c_f32_bf16 v229, s77, v160\n"
    "v_dot2c_f32_bf16 v225, s77, v194\n"
    "v_dot2c_f32_bf16 v227, v194, v194\n"
    "v_dot2c_f32_bf16 v226, s77, v195\n"
    "v_dot2c_f32_bf16 v228, v195, v195\n"
    "v_dot2c_f32_bf16 v230, s77, v176\n"
    "v_dot2c_f32_bf16 v225, s77, v196\n"
    "v_dot2c_f32_bf16 v227, v196, v196\n"
    "v_dot2c_f32_bf16 v226, s77, v197\n"
    "v_dot2c_f32_bf16 v228, v197, v197\n"
    "v_dot2c_f32_bf16 v229, s77, v161\n"
    "v_dot2c_f32_bf16 v225, s77, v198\n"
    "v_dot2c_f32_bf16 v227, v198, v198\n"
    "v_dot2c_f32_bf16 v226, s77, v199\n"
    "v_dot2c_f32_bf16 v228, v199, v199\n"
    "v_dot2c_f32_bf16 v230, s77, v177\n"
    "v_dot2c_f32_bf16 v225, s77, v200\n"
    "v_dot2c_f32_bf16 v227, v200, v200\n"
    "v_dot2c_f32_bf16 v226, s77, v201\n"
    "v_dot2c_f32_bf16 v228, v201, v201\n"
    "v_dot2c_f32_bf16 v229, s77, v162\n"
    "v_dot2c_f32_bf16 v225, s77, v202\n"
    "v_dot2c_f32_bf16 v227, v202, v202\n"
    "v_dot2c_f32_bf16 v226, s77, v203\n"
    "v_dot2c_f32_bf16 v228, v203, v203\n"
    "v_dot2c_f32_bf16 v230, s77, v178\n"
    "v_dot2c_f32_bf16 v225, s77, v204\n"
    "v_dot2c_f32_bf16 v227, v204, v204\n"
    "v_dot2c_f32_bf16 v226, s77, v205\n"
    "v_dot2c_f32_bf16 v228, v205, v205\n"
    "v_dot2c_f32_bf16 v229, s77, v163\n"
    "v_dot2c_f32_bf16 v225, s77, v206\n"
    "v_dot2c_f32_bf16 v227, v206, v206\n"
    "v_dot2c_f32_bf16 v226, s77, v207\n"
    "v_dot2c_f32_bf16 v228, v207, v207\n"
    "v_dot2c_f32_bf16 v230, s77, v179\n"
    "s_waitcnt vmcnt(6) lgkmcnt(0)\n"
    "s_barrier\n"
    "v_mfma_f32_32x32x16_bf16 v[96:111], a[48:51], v[160:163], v[96:111]\n"
    "ds_read_b128 v[192:195], v93 offset:24576\n"
    "ds_read_b128 v[196:199], v94 offset:24576\n"
    "s_add_u32 s76, s75, 768\n"
    "s_and_b32 s76, s76, 1023\n"
    "s_add_u32 m0, s72, 0\n"
    "v_xor_b32 v28, v28, v249\n"
    "v_mfma_f32_32x32x16_bf16 v[112:127], a[48:51], v[176:179], v[112:127]\n"
    "ds_read_b128 v[200:203], v95 offset:24576\n"
    "ds_read_b128 v[204:207], v224 offset:24576\n"
    "s_add_u32 s74, s66, s76\n"
    "buffer_load_dwordx4 v80, s[4:7], s74 offen offset:0 lds\n"
    "v_mul_f32 v28, v248, v28\n"
    "v_mul_f32 v30, v29, v241\n"
    "v_mfma_f32_32x32x16_bf16 v[96:111], a[52:55], v[164:167], v[96:111]\n"
    "ds_read_b128 v[128:131], v89 offset:24576\n"
    "ds_read_b128 v[144:147], v89 offset:28672\n"
    "buffer_load_dwordx4 v81, s[4:7], s74 offen offset:1024 lds\n"
    "v_add_f32 v244, -1.0, v28\n"
    "v_mul_f32 v31, v29, v240\n"
    "v_fmac_f32 v30, v244, v240\n"
    "v_mfma_f32_32x32x16_bf16 v[112:127], a[52:55], v[180:183], v[112:127]\n"
    "ds_read_b128 a[64:67], v93 offset:24576\n"
    "ds_read_b128 a[68:71], v94 offset:24576\n"
    "buffer_load_dwordx4 v82, s[4:7], s74 offen offset:2048 lds\n"
    "v_fma_f32 v31, -v244, v241, v31\n"
    "v_mul_f32 v30, v30, v242\n"
    "v_mul_f32 v31, v31, v242\n"
    "v_mfma_f32_32x32x16_bf16 v[96:111], a[56:59], v[168:171], v[96:111]\n"
    "ds_read_b128 a[72:75], v95 offset:24576\n"
    "ds_read_b128 a[76:79], v224 offset:24576\n"
    "buffer_load_dwordx4 v83, s[4:7], s74 offen offset:3072 lds\n"
    "s_add_u32 m0, s73, 0\n"
    "s_add_u32 s74, s67, s76\n"
    "v_readfirstlane_b32 s86, v235\n"
    "v_mfma_f32_32x32x16_bf16 v[112:127], a[56:59], v[184:187], v[112:127]\n"
    "ds_read_b128 v[132:135], v90 offset:24576\n"
    "ds_read_b128 v[148:151], v90 offset:28672\n"
    "buffer_load_dwordx4 v84, s[8:11], s74 offen offset:0 lds\n"
    "v_readfirstlane_b32 s84, v236\n"
    "v_readfirstlane_b32 s85, v234\n"
    "s_add_u32 s86, s86, 1\n"
    "v_mfma_f32_32x32x16_bf16 v[96:111], a[60:63], v[172:175], v[96:111]\n"
    "ds_read_b128 v[136:139], v91 offset:24576\n"
    "ds_read_b128 v[152:155], v91 offset:28672\n"
    "buffer_load_dwordx4 v85, s[8:11], s74 offen offset:1024 lds\n"
    "s_add_u32 s84, s84, 1\n"
    "v_mov_b32 v55, s86\n"
    "v_mov_b32 v32, v28\n"
    "v_mfma_f32_32x32x16_bf16 v[112:127], a[60:63], v[188:191], v[112:127]\n"
    "ds_read_b128 v[140:143], v92 offset:24576\n"
    "ds_read_b128 v[156:159], v92 offset:28672\n"
    "v_mov_b32 v33, v29\n"
    "v_mul_f32 v1, v33, v33\n"
    "v_mul_f32 v33, v32, v33\n"
    "v_fma_f32 v32, v32, v32, -v1\n"
    "s_waitcnt lgkmcnt(10)\n"
    "v_dot2c_f32_bf16 v225, s77, v192\n"
    "v_dot2c_f32_bf16 v227, v192, v192\n"
    "v_dot2c_f32_bf16 v226, s77, v193\n"
    "v_dot2c_f32_bf16 v228, v193, v193\n"
    "v_dot2c_f32_bf16 v229, s77, v128\n"
    "v_dot2c_f32_bf16 v225, s77, v194\n"
    "v_dot2c_f32_bf16 v227, v194, v194\n"
    "v_dot2c_f32_bf16 v226, s77, v195\n"
    "v_dot2c_f32_bf16 v228, v195, v195\n"
    "v_dot2c_f32_bf16 v230, s77, v144\n"
    "v_dot2c_f32_bf16 v225, s77, v196\n"
    "v_dot2c_f32_bf16 v227, v196, v196\n"
    "v_dot2c_f32_bf16 v226, s77, v197\n"
    "v_dot2c_f32_bf16 v228, v197, v197\n"
    "v_dot2c_f32_bf16 v229, s77, v129\n"
    "v_dot2c_f32_bf16 v225, s77, v198\n"
    "v_dot2c_f32_bf16 v227, v198, v198\n"
    "v_dot2c_f32_bf16 v226, s77, v199\n"
    "v_dot2c_f32_bf16 v228, v199, v199\n"
    "v_dot2c_f32_bf16 v230, s77, v145\n"
    "v_dot2c_f32_bf16 v225, s77, v200\n"
    "v_dot2c_f32_bf16 v227, v200, v200\n"
    "v_dot2c_f32_bf16 v226, s77, v201\n"
    "v_dot2c_f32_bf16 v228, v201, v201\n"
    "v_dot2c_f32_bf16 v229, s77, v130\n"
    "v_dot2c_f32_bf16 v225, s77, v202\n"
    "v_dot2c_f32_bf16 v227, v202, v202\n"
    "v_dot2c_f32_bf16 v226, s77, v203\n"
    "v_dot2c_f32_bf16 v228, v203, v203\n"
    "v_dot2c_f32_bf16 v230, s77, v146\n"
    "v_dot2c_f32_bf16 v225, s77, v204\n"
    "v_dot2c_f32_bf16 v227, v204, v204\n"
    "v_dot2c_f32_bf16 v226, s77, v205\n"
    "v_dot2c_f32_bf16 v228, v205, v205\n"
    "v_dot2c_f32_bf16 v229, s77, v131\n"
    "v_dot2c_f32_bf16 v225, s77, v206\n"
    "v_dot2c_f32_bf16 v227, v206, v206\n"
    "v_dot2c_f32_bf16 v226, s77, v207\n"
    "v_dot2c_f32_bf16 v228, v207, v207\n"
    "v_dot2c_f32_bf16 v230, s77, v147\n"
    "s_waitcnt vmcnt(6) lgkmcnt(0)\n"
    "s_barrier\n"
    "v_mfma_f32_32x32x16_bf16 v[96:111], a[64:67], v[128:131], v[96:111]\n"
    "ds_read_b128 v[192:195], v93 offset:49152\n"
    "ds_read_b128 v[196:199], v94 offset:49152\n"
    "s_add_u32 s76, s75, 896\n"
    "s_and_b32 s76, s76, 1023\n"
    "s_add_u32 m0, s72, 24576\n"
    "v_add_f32 v33, v33, v33\n"
    "v_mfma_f32_32x32x16_bf16 v[112:127], a[64:67], v[144:147], v[112:127]\n"
    "ds_read_b128 v[200:203], v95 offset:49152\n"
    "ds_read_b128 v[204:207], v224 offset:49152\n"
    "s_add_u32 s74, s66, s76\n"
    "buffer_load_dwordx4 v80, s[4:7], s74 offen offset:0 lds\n"
    "buffer_load_dwordx4 v81, s[4:7], s74 offen offset:1024 lds\n"
    "v_mul_f32 v1, v33, v33\n"
    "v_mfma_f32_32x32x16_bf16 v[96:111], a[68:71], v[132:135], v[96:111]\n"
    "ds_read_b128 v[160:163], v89 offset:49152\n"
    "ds_read_b128 v[176:179], v89 offset:53248\n"
    "buffer_load_dwordx4 v82, s[4:7], s74 offen offset:2048 lds\n"
    "v_mul_f32 v33, v32, v33\n"
    "v_fma_f32 v32, v32, v32, -v1\n"
    "v_add_f32 v33, v33, v33\n"
    "v_mfma_f32_32x32x16_bf16 v[112:127], a[68:71], v[148:151], v[112:127]\n"
    "ds_read_b128 a[80:83], v93 offset:49152\n"
    "ds_read_b128 a[84:87], v94 offset:49152\n"
    "buffer_load_dwordx4 v83, s[4:7], s74 offen offset:3072 lds\n"
    "s_add_u32 m0, s73, 24576\n"
    "s_add_u32 s74, s67, s76\n"
    "v_mul_f32 v1, v33, v33\n"
    "v_mfma_f32_32x32x16_bf16 v[96:111], a[72:75], v[136:139], v[96:111]\n"
    "ds_read_b128 a[88:91], v95 offset:49152\n"
    "ds_read_b128 a[92:95], v224 offset:49152\n"
    "buffer_load_dwordx4 v84, s[8:11], s74 offen offset:0 lds\n"
    "v_mul_f32 v33, v32, v33\n"
    "v_fma_f32 v32, v32, v32, -v1\n"
    "v_add_f32 v33, v33, v33\n"
    "v_mfma_f32_32x32x16_bf16 v[112:127], a[72:75], v[152:155], v[112:127]\n"
    "ds_read_b128 v[164:167], v90 offset:49152\n"
    "ds_read_b128 v[180:183], v90 offset:53248\n"
    "buffer_load_dwordx4 v85, s[8:11], s74 offen offset:1024 lds\n"
    "s_add_u32 m0, s40, 81920\n"
    "s_add_u32 s74, s12, 0\n"
    "v_mul_f32 v1, v33, v33\n"
    "v_mfma_f32_32x32x16_bf16 v[96:111], a[76:79], v[140:143], v[96:111]\n"
    "ds_read_b128 v[168:171], v91 offset:49152\n"
    "ds_read_b128 v[184:187], v91 offset:53248\n"
    "buffer_load_dwordx4 v15, s[16:19], s74 offen offset:0 lds\n"
    "v_mul_f32 v33, v32, v33\n"
    "v_fma_f32 v32, v32, v32, -v1\n"
    "v_add_f32 v33, v33, v33\n"
    "v_mfma_f32_32x32x16_bf16 v[112:127], a[76:79], v[156:159], v[112:127]\n"
    "ds_read_b128 v[172:175], v92 offset:49152\n"
    "ds_read_b128 v[188:191], v92 offset:53248\n"
    "buffer_load_dwordx4 v16, s[16:19], s74 offen offset:1024 lds\n"
    "buffer_load_dwordx4 v17, s[16:19], s74 offen offset:2048 lds\n"
    "v_mov_b32 v34, v32\n"
    "v_mov_b32 v35, v33\n"
    "buffer_load_dwordx4 v18, s[16:19], s74 offen offset:3072 lds\n"
    "s_waitcnt lgkmcnt(10)\n"
    "v_dot2c_f32_bf16 v225, s77, v192\n"
    "v_dot2c_f32_bf16 v227, v192, v192\n"
    "v_dot2c_f32_bf16 v226, s77, v193\n"
    "v_dot2c_f32_bf16 v228, v193, v193\n"
    "v_dot2c_f32_bf16 v229, s77, v160\n"
    "v_dot2c_f32_bf16 v225, s77, v194\n"
    "v_dot2c_f32_bf16 v227, v194, v194\n"
    "v_dot2c_f32_bf16 v226, s77, v195\n"
    "v_dot2c_f32_bf16 v228, v195, v195\n"
    "v_dot2c_f32_bf16 v230, s77, v176\n"
    "v_dot2c_f32_bf16 v225, s77, v196\n"
    "v_dot2c_f32_bf16 v227, v196, v196\n"
    "v_dot2c_f32_bf16 v226, s77, v197\n"
    "v_dot2c_f32_bf16 v228, v197, v197\n"
    "v_dot2c_f32_bf16 v229, s77, v161\n"
    "v_dot2c_f32_bf16 v225, s77, v198\n"
    "v_dot2c_f32_bf16 v227, v198, v198\n"
    "v_dot2c_f32_bf16 v226, s77, v199\n"
    "v_dot2c_f32_bf16 v228, v199, v199\n"
    "v_dot2c_f32_bf16 v230, s77, v177\n"
    "v_dot2c_f32_bf16 v225, s77, v200\n"
    "v_dot2c_f32_bf16 v227, v200, v200\n"
    "v_dot2c_f32_bf16 v226, s77, v201\n"
    "v_dot2c_f32_bf16 v228, v201, v201\n"
    "v_dot2c_f32_bf16 v229, s77, v162\n"
    "v_dot2c_f32_bf16 v225, s77, v202\n"
    "v_dot2c_f32_bf16 v227, v202, v202\n"
    "v_dot2c_f32_bf16 v226, s77, v203\n"
    "v_dot2c_f32_bf16 v228, v203, v203\n"
    "v_dot2c_f32_bf16 v230, s77, v178\n"
    "v_dot2c_f32_bf16 v225, s77, v204\n"
    "v_dot2c_f32_bf16 v227, v204, v204\n"
    "v_dot2c_f32_bf16 v226, s77, v205\n"
    "v_dot2c_f32_bf16 v228, v205, v205\n"
    "v_dot2c_f32_bf16 v229, s77, v163\n"
    "v_dot2c_f32_bf16 v225, s77, v206\n"
    "v_dot2c_f32_bf16 v227, v206, v206\n"
    "v_dot2c_f32_bf16 v226, s77, v207\n"
    "v_dot2c_f32_bf16 v228, v207, v207\n"
    "v_dot2c_f32_bf16 v230, s77, v179\n"
    "s_waitcnt vmcnt(10) lgkmcnt(0)\n"
    "s_barrier\n"
    "v_mfma_f32_32x32x16_bf16 v[96:111], a[80:83], v[160:163], v[96:111]\n"
    "ds_read_b128 v[192:195], v93 offset:0\n"
    "ds_read_b128 v[196:199], v94 offset:0\n"
    "s_add_u32 m0, s40, 86016\n"
    "s_add_u32 s74, s12, 114688\n"
    "buffer_load_dwordx4 v15, s[16:19], s74 offen offset:0 lds\n"
    "v_cmp_eq_u32 vcc, 1, v8\n"
    "s_lshl_b32 s68, s60, 1\n"
    "v_add_u32 v215, s68, v8\n"
    "v_cndmask_b32 v36, 1.0, v32, vcc\n"
    "v_cndmask_b32 v37, 0, v33, vcc\n"
    "v_mfma_f32_32x32x16_bf16 v[112:127], a[80:83], v[176:179], v[112:127]\n"
    "ds_read_b128 v[200:203], v95 offset:0\n"
    "ds_read_b128 v[204:207], v224 offset:0\n"
    "buffer_load_dwordx4 v16, s[16:19], s74 offen offset:1024 lds\n"
    "v_mul_f32 v1, v35, v35\n"
    "v_mul_f32 v35, v34, v35\n"
    "v_fma_f32 v34, v34, v34, -v1\n"
    "v_mfma_f32_32x32x16_bf16 v[96:111], a[84:87], v[164:167], v[96:111]\n"
    "ds_read_b128 v[128:131], v89 offset:0\n"
    "ds_read_b128 v[144:147], v89 offset:4096\n"
    "buffer_load_dwordx4 v17, s[16:19], s74 offen offset:2048 lds\n"
    "v_add_f32 v35, v35, v35\n"
    "s_bitcmp1_b32 s60, 0\n"
    "s_cselect_b64 vcc, -1, 0\n"
    "v_cndmask_b32 v2, 1.0, v34, vcc\n"
    "v_cndmask_b32 v3, 0, v35, vcc\n"
    "v_mfma_f32_32x32x16_bf16 v[112:127], a[84:87], v[180:183], v[112:127]\n"
    "ds_read_b128 a[96:99], v93 offset:0\n"
    "ds_read_b128 a[100:103], v94 offset:0\n"
    "buffer_load_dwordx4 v18, s[16:19], s74 offen offset:3072 lds\n"
    "s_add_u32 m0, s40, 114688\n"
    "s_add_u32 s74, s13, 0\n"
    "v_mul_f32 v1, v37, v3\n"
    "v_mfma_f32_32x32x16_bf16 v[96:111], a[88:91], v[168:171], v[96:111]\n"
    "ds_read_b128 a[104:107], v95 offset:0\n"
    "ds_read_b128 a[108:111], v224 offset:0\n"
    "buffer_load_dwordx4 v15, s[16:19], s74 offen offset:0 lds\n"
    "v_mul_f32 v3, v36, v3\n"
    "v_fma_f32 v36, v36, v2, -v1\n"
    "v_fma_f32 v37, v37, v2, v3\n"
    "v_mfma_f32_32x32x16_bf16 v[112:127], a[88:91], v[184:187], v[112:127]\n"
    "ds_read_b128 v[132:135], v90 offset:0\n"
    "ds_read_b128 v[148:151], v90 offset:4096\n"
    "buffer_load_dwordx4 v16, s[16:19], s74 offen offset:1024 lds\n"
    "v_mul_f32 v1, v35, v35\n"
    "v_mul_f32 v35, v34, v35\n"
    "v_fma_f32 v34, v34, v34, -v1\n"
    "v_mfma_f32_32x32x16_bf16 v[96:111], a[92:95], v[172:175], v[96:111]\n"
    "ds_read_b128 v[136:139], v91 offset:0\n"
    "ds_read_b128 v[152:155], v91 offset:4096\n"
    "buffer_load_dwordx4 v17, s[16:19], s74 offen offset:2048 lds\n"
    "v_add_f32 v35, v35, v35\n"
    "s_bitcmp1_b32 s60, 1\n"
    "s_cselect_b64 vcc, -1, 0\n"
    "v_cndmask_b32 v2, 1.0, v34, vcc\n"
    "v_cndmask_b32 v3, 0, v35, vcc\n"
    "v_mfma_f32_32x32x16_bf16 v[112:127], a[92:95], v[188:191], v[112:127]\n"
    "ds_read_b128 v[140:143], v92 offset:0\n"
    "ds_read_b128 v[156:159], v92 offset:4096\n"
    "buffer_load_dwordx4 v18, s[16:19], s74 offen offset:3072 lds\n"
    "v_mul_f32 v1, v37, v3\n"
    "v_mul_f32 v3, v36, v3\n"
    "v_fma_f32 v36, v36, v2, -v1\n"
    "s_waitcnt lgkmcnt(10)\n"
    "v_dot2c_f32_bf16 v225, s77, v192\n"
    "v_dot2c_f32_bf16 v227, v192, v192\n"
    "v_dot2c_f32_bf16 v226, s77, v193\n"
    "v_dot2c_f32_bf16 v228, v193, v193\n"
    "v_dot2c_f32_bf16 v229, s77, v128\n"
    "v_dot2c_f32_bf16 v225, s77, v194\n"
    "v_dot2c_f32_bf16 v227, v194, v194\n"
    "v_dot2c_f32_bf16 v226, s77, v195\n"
    "v_dot2c_f32_bf16 v228, v195, v195\n"
    "v_dot2c_f32_bf16 v230, s77, v144\n"
    "v_dot2c_f32_bf16 v225, s77, v196\n"
    "v_dot2c_f32_bf16 v227, v196, v196\n"
    "v_dot2c_f32_bf16 v226, s77, v197\n"
    "v_dot2c_f32_bf16 v228, v197, v197\n"
    "v_dot2c_f32_bf16 v229, s77, v129\n"
    "v_dot2c_f32_bf16 v225, s77, v198\n"
    "v_dot2c_f32_bf16 v227, v198, v198\n"
    "v_dot2c_f32_bf16 v226, s77, v199\n"
    "v_dot2c_f32_bf16 v228, v199, v199\n"
    "v_dot2c_f32_bf16 v230, s77, v145\n"
    "v_dot2c_f32_bf16 v225, s77, v200\n"
    "v_dot2c_f32_bf16 v227, v200, v200\n"
    "v_dot2c_f32_bf16 v226, s77, v201\n"
    "v_dot2c_f32_bf16 v228, v201, v201\n"
    "v_dot2c_f32_bf16 v229, s77, v130\n"
    "v_dot2c_f32_bf16 v225, s77, v202\n"
    "v_dot2c_f32_bf16 v227, v202, v202\n"
    "v_dot2c_f32_bf16 v226, s77, v203\n"
    "v_dot2c_f32_bf16 v228, v203, v203\n"
    "v_dot2c_f32_bf16 v230, s77, v146\n"
    "v_dot2c_f32_bf16 v225, s77, v204\n"
    "v_dot2c_f32_bf16 v227, v204, v204\n"
    "v_dot2c_f32_bf16 v226, s77, v205\n"
    "v_dot2c_f32_bf16 v228, v205, v205\n"
    "v_dot2c_f32_bf16 v229, s77, v131\n"
    "v_dot2c_f32_bf16 v225, s77, v206\n"
    "v_dot2c_f32_bf16 v227, v206, v206\n"
    "v_dot2c_f32_bf16 v226, s77, v207\n"
    "v_dot2c_f32_bf16 v228, v207, v207\n"
    "v_dot2c_f32_bf16 v230, s77, v147\n"
    "s_waitcnt vmcnt(12) lgkmcnt(0)\n"
    "s_barrier\n"
    "v_mfma_f32_32x32x16_bf16 v[96:111], a[96:99], v[128:131], v[96:111]\n"
    "ds_read_b128 v[192:195], v93 offset:24576\n"
    "ds_read_b128 v[196:199], v94 offset:24576\n"
    "s_add_u32 m0, s40, 118784\n"
    "s_add_u32 s74, s13, 114688\n"
    "buffer_load_dwordx4 v15, s[16:19], s74 offen offset:0 lds\n"
    "v_fma_f32 v37, v37, v2, v3\n"
    "v_mfma_f32_32x32x16_bf16 v[112:127], a[96:99], v[144:147], v[112:127]\n"
    "ds_read_b128 v[200:203], v95 offset:24576\n"
    "ds_read_b128 v[204:207], v224 offset:24576\n"
    "buffer_load_dwordx4 v16, s[16:19], s74 offen offset:1024 lds\n"
    "v_mul_f32 v1, v35, v35\n"
    "v_mul_f32 v35, v34, v35\n"
    "v_fma_f32 v34, v34, v34, -v1\n"
    "v_mfma_f32_32x32x16_bf16 v[96:111], a[100:103], v[132:135], v[96:111]\n"
    "ds_read_b128 v[160:163], v89 offset:24576\n"
    "ds_read_b128 v[176:179], v89 offset:28672\n"
    "buffer_load_dwordx4 v17, s[16:19], s74 offen offset:2048 lds\n"
    "v_add_f32 v35, v35, v35\n"
    "v_mfma_f32_32x32x16_bf16 v[112:127], a[100:103], v[148:151], v[112:127]\n"
    "ds_read_b128 a[112:115], v93 offset:24576\n"
    "ds_read_b128 a[116:119], v94 offset:24576\n"
    "buffer_load_dwordx4 v18, s[16:19], s74 offen offset:3072 lds\n"
    "v_mfma_f32_32x32x16_bf16 v[96:111], a[104:107], v[136:139], v[96:111]\n"
    "ds_read_b128 a[120:123], v95 offset:24576\n"
    "ds_read_b128 a[124:127], v224 offset:24576\n"
    "buffer_load_dword v6, v239, s[20:23], s82 offen\n"
    "v_mfma_f32_32x32x16_bf16 v[112:127], a[104:107], v[152:155], v[112:127]\n"
    "ds_read_b128 v[164:167], v90 offset:24576\n"
    "ds_read_b128 v[180:183], v90 offset:28672\n"
    "buffer_load_dword v6, v239, s[24:27], s83 offen\n"
    "v_mfma_f32_32x32x16_bf16 v[96:111], a[108:111], v[140:143], v[96:111]\n"
    "ds_read_b128 v[168:171], v91 offset:24576\n"
    "ds_read_b128 v[184:187], v91 offset:28672\n"
    "v_mfma_f32_32x32x16_bf16 v[112:127], a[108:111], v[156:159], v[112:127]\n"
    "ds_read_b128 v[172:175], v92 offset:24576\n"
    "ds_read_b128 v[188:191], v92 offset:28672\n"
    "s_waitcnt lgkmcnt(10)\n"
    "v_dot2c_f32_bf16 v225, s77, v192\n"
    "v_dot2c_f32_bf16 v227, v192, v192\n"
    "v_dot2c_f32_bf16 v226, s77, v193\n"
    "v_dot2c_f32_bf16 v228, v193, v193\n"
    "v_dot2c_f32_bf16 v229, s77, v160\n"
    "v_dot2c_f32_bf16 v225, s77, v194\n"
    "v_dot2c_f32_bf16 v227, v194, v194\n"
    "v_dot2c_f32_bf16 v226, s77, v195\n"
    "v_dot2c_f32_bf16 v228, v195, v195\n"
    "v_dot2c_f32_bf16 v230, s77, v176\n"
    "v_dot2c_f32_bf16 v225, s77, v196\n"
    "v_dot2c_f32_bf16 v227, v196, v196\n"
    "v_dot2c_f32_bf16 v226, s77, v197\n"
    "v_dot2c_f32_bf16 v228, v197, v197\n"
    "v_dot2c_f32_bf16 v229, s77, v161\n"
    "v_dot2c_f32_bf16 v225, s77, v198\n"
    "v_dot2c_f32_bf16 v227, v198, v198\n"
    "v_dot2c_f32_bf16 v226, s77, v199\n"
    "v_dot2c_f32_bf16 v228, v199, v199\n"
    "v_dot2c_f32_bf16 v230, s77, v177\n"
    "v_dot2c_f32_bf16 v225, s77, v200\n"
    "v_dot2c_f32_bf16 v227, v200, v200\n"
    "v_dot2c_f32_bf16 v226, s77, v201\n"
    "v_dot2c_f32_bf16 v228, v201, v201\n"
    "v_dot2c_f32_bf16 v229, s77, v162\n"
    "v_dot2c_f32_bf16 v225, s77, v202\n"
    "v_dot2c_f32_bf16 v227, v202, v202\n"
    "v_dot2c_f32_bf16 v226, s77, v203\n"
    "v_dot2c_f32_bf16 v228, v203, v203\n"
    "v_dot2c_f32_bf16 v230, s77, v178\n"
    "v_dot2c_f32_bf16 v225, s77, v204\n"
    "v_dot2c_f32_bf16 v227, v204, v204\n"
    "v_dot2c_f32_bf16 v226, s77, v205\n"
    "v_dot2c_f32_bf16 v228, v205, v205\n"
    "v_dot2c_f32_bf16 v229, s77, v163\n"
    "v_dot2c_f32_bf16 v225, s77, v206\n"
    "v_dot2c_f32_bf16 v227, v206, v206\n"
    "v_dot2c_f32_bf16 v226, s77, v207\n"
    "v_dot2c_f32_bf16 v228, v207, v207\n"
    "v_dot2c_f32_bf16 v230, s77, v179\n"
    "ds_write_b32 v45, v229\n"
    "s_nop 1\n"
    "ds_write_b32 v45, v230 offset:1024\n"
    "s_mov_b32 s68, 0x3b000000\n"
    "s_mov_b32 s69, 0x01000100\n"
    "s_lshl_b32 s70, s60, 7\n"
    "s_add_u32 s70, s70, 154624\n"
    "s_mov_b32 s71, 0xffff\n"
    "s_waitcnt lgkmcnt(11)\n"
    "v_mfma_f32_32x32x16_bf16 v[96:111], a[112:115], v[160:163], v[96:111]\n"
    "v_add_f32 v225, v225, v226\n"
    "v_add_f32 v227, v227, v228\n"
    "v_mov_b32 v226, v225\n"
    "v_mov_b32 v228, v227\n"
    "v_lshlrev_b32 v1, 2, v7\n"
    "v_lshrrev_b32 v2, 1, v7\n"
    "v_permlane32_swap_b32 v225, v226\n"
    "v_permlane32_swap_b32 v227, v228\n"
    "v_and_b32 v1, 16, v1\n"
    "v_mfma_f32_32x32x16_bf16 v[112:127], a[112:115], v[176:179], v[112:127]\n"
    "v_and_or_b32 v1, v2, 12, v1\n"
    "v_add_f32 v225, v225, v226\n"
    "v_add_f32 v227, v227, v228\n"
    "v_mul_f32 v225, 0x3b000000, v225\n"
    "v_and_or_b32 v1, v7, 3, v1\n"
    "v_mul_f32 v226, v225, v225\n"
    "v_lshlrev_b32 v1, 2, v1\n"
    "v_fma_f32 v227, v227, s68, -v226\n"
    "v_max_f32 v227, 0, v227\n"
    "s_waitcnt lgkmcnt(7)\n"
    "v_mfma_f32_32x32x16_bf16 v[96:111], a[116:119], v[164:167], v[96:111]\n"
    "v_cvt_pk_bf16_f32 v227, v225, v227\n"
    "v_and_b32 v228, 0xffff0000, v227\n"
    "v_xor_b32 v225, 0x8000, v227\n"
    "v_add_f32 v228, 0x3727c5ac, v228\n"
    "v_perm_b32 v56, v225, v225, s69\n"
    "v_cvt_pk_bf16_f32 v228, v228, v228\n"
    "v_lshl_add_u32 v1, v8, 9, v1\n"
    "v_and_b32 v228, 0xffff0000, v228\n"
    "v_sqrt_f32 v228, v228\n"
    "s_waitcnt lgkmcnt(6)\n"
    "v_mfma_f32_32x32x16_bf16 v[112:127], a[116:119], v[180:183], v[112:127]\n"
    "v_mov_b32 v58, 0\n"
    "v_cvt_pk_bf16_f32 v228, v228, v228\n"
    "v_add_u32 v1, s70, v1\n"
    "v_and_b32 v228, 0xffff0000, v228\n"
    "v_rcp_f32 v64, v228\n"
    "v_bfi_b32 v57, s71, v225, v228\n"
    "v_lshlrev_b32 v227, 16, v227\n"
    "v_cmp_eq_u32 vcc, 0, v8\n"
    "v_mov_b32 v59, 0\n"
    "v_mov_b32 v62, 0\n"
    "v_cndmask_b32 v227, v227, v64, vcc\n"
    "ds_write_b32 v1, v227\n"
    "v_mov_b32 v63, 0\n"
    "s_waitcnt vmcnt(2) lgkmcnt(0)\n"
    "s_barrier\n"
    "v_mfma_f32_32x32x16_bf16 v[96:111], a[120:123], v[168:171], v[96:111]\n"
    "ds_read_b64_tr_b16 v[128:129], v27 offset:0\n"
    "ds_read_b64_tr_b16 v[130:131], v27 offset:512\n"
    "ds_read_b128 v[248:251], v43\n"
    "ds_read_b128 v[252:255], v43 offset:1024\n"
    "ds_read_b128 v[144:147], v44 offset:0\n"
    "ds_read_b128 v[148:151], v44 offset:16\n"
    "ds_read_b128 v[152:155], v44 offset:32\n"
    "ds_read_b128 v[156:159], v44 offset:48\n"
    "s_waitcnt lgkmcnt(5)\n"
    "v_add_f32 v248, v248, v249\n"
    "v_add_f32 v250, v250, v251\n"
    "v_mfma_f32_32x32x16_bf16 v[112:127], a[120:123], v[184:187], v[112:127]\n"
    "ds_read_b64_tr_b16 v[132:133], v27 offset:8192\n"
    "ds_read_b64_tr_b16 v[134:135], v27 offset:8704\n"
    "v_add_f32 v248, v248, v250\n"
    "v_and_b32 v249, 0xffff0000, v248\n"
    "v_sub_f32 v249, v248, v249\n"
    "v_and_b32 v250, 0xffff0000, v249\n"
    "v_perm_b32 v60, v249, v248, s29\n"
    "v_sub_f32 v250, v249, v250\n"
    "v_lshrrev_b32 v61, 16, v250\n"
    "s_nop 1\n"
    "v_mfma_f32_32x32x16_bf16 v[96:111], v[56:59], v[60:63], v[96:111]\n"
    "v_mfma_f32_32x32x16_bf16 v[96:111], a[124:127], v[172:175], v[96:111]\n"
    "ds_read_b64_tr_b16 v[136:137], v27 offset:16384\n"
    "ds_read_b64_tr_b16 v[138:139], v27 offset:16896\n"
    "s_waitcnt lgkmcnt(8)\n"
    "v_add_f32 v252, v252, v253\n"
    "v_add_f32 v254, v254, v255\n"
    "v_add_f32 v252, v252, v254\n"
    "v_and_b32 v253, 0xffff0000, v252\n"
    "v_sub_f32 v253, v252, v253\n"
    "v_and_b32 v254, 0xffff0000, v253\n"
    "v_perm_b32 v60, v253, v252, s29\n"
    "v_sub_f32 v254, v253, v254\n"
    "v_mfma_f32_32x32x16_bf16 v[112:127], a[124:127], v[188:191], v[112:127]\n"
    "ds_read_b64_tr_b16 v[140:141], v27 offset:24576\n"
    "ds_read_b64_tr_b16 v[142:143], v27 offset:25088\n"
    "v_lshrrev_b32 v61, 16, v254\n"
    "s_nop 1\n"
    "v_mfma_f32_32x32x16_bf16 v[112:127], v[56:59], v[60:63], v[112:127]\n"
    "s_waitcnt lgkmcnt(4)\n"
    "v_mfma_f32_32x32x16_bf16 v[192:207], v[128:131], a[0:3], 0\n"
    "ds_read_b64_tr_b16 v[224:225], v38 offset:0\n"
    "ds_read_b64_tr_b16 v[226:227], v38 offset:512\n"
    "v_mul_f32 v160, v144, v96\n"
    "v_mul_f32 v162, v145, v97\n"
    "v_mul_f32 v164, v146, v98\n"
    "v_mul_f32 v166, v147, v99\n"
    "v_mul_f32 v168, v148, v100\n"
    "v_mul_f32 v170, v149, v101\n"
    "v_mul_f32 v172, v150, v102\n"
    "v_mfma_f32_32x32x16_bf16 v[192:207], v[132:135], a[16:19], v[192:207]\n"
    "ds_read_b64_tr_b16 v[228:229], v38 offset:8192\n"
    "ds_read_b64_tr_b16 v[230:231], v38 offset:8704\n"
    "v_mul_f32 v174, v151, v103\n"
    "v_mul_f32 v176, v152, v104\n"
    "v_mul_f32 v178, v153, v105\n"
    "v_mul_f32 v180, v154, v106\n"
    "v_mul_f32 v182, v155, v107\n"
    "v_mul_f32 v184, v156, v108\n"
    "v_mul_f32 v186, v157, v109\n"
    "v_mul_f32 v188, v158, v110\n"
    "s_waitcnt lgkmcnt(4)\n"
    "v_mfma_f32_32x32x16_bf16 v[192:207], v[136:139], a[32:35], v[192:207]\n"
    "ds_read_b64_tr_b16 v[232:233], v38 offset:16384\n"
    "ds_read_b64_tr_b16 v[234:235], v38 offset:16896\n"
    "v_mul_f32 v190, v159, v111\n"
    "v_mul_f32 v161, v144, v112\n"
    "v_mul_f32 v163, v145, v113\n"
    "v_mul_f32 v165, v146, v114\n"
    "v_mul_f32 v167, v147, v115\n"
    "v_mul_f32 v169, v148, v116\n"
    "v_mul_f32 v171, v149, v117\n"
    "v_mfma_f32_32x32x16_bf16 v[192:207], v[140:143], a[48:51], v[192:207]\n"
    "ds_read_b64_tr_b16 v[236:237], v38 offset:24576\n"
    "ds_read_b64_tr_b16 v[238:239], v38 offset:25088\n"
    "v_mul_f32 v173, v150, v118\n"
    "v_mul_f32 v175, v151, v119\n"
    "v_mul_f32 v177, v152, v120\n"
    "v_mul_f32 v179, v153, v121\n"
    "v_mul_f32 v181, v154, v122\n"
    "v_mul_f32 v183, v155, v123\n"
    "v_mul_f32 v185, v156, v124\n"
    "v_mul_f32 v187, v157, v125\n"
    "s_waitcnt lgkmcnt(4)\n"
    "v_mfma_f32_32x32x16_bf16 v[192:207], v[224:227], a[4:7], v[192:207]\n"
    "ds_read_b64_tr_b16 v[240:241], v39 offset:0\n"
    "ds_read_b64_tr_b16 v[242:243], v39 offset:512\n"
    "v_mul_f32 v189, v158, v126\n"
    "v_mul_f32 v191, v159, v127\n"
    "v_pk_fma_f32 v[70:71], v[160:161], v[28:29], v[162:163] op_sel_hi:[1,0,1]\n"
    "v_pk_fma_f32 v[70:71], v[160:161], v[28:29], v[70:71] op_sel:[1,1,0] op_sel_hi:[0,1,1] neg_lo:[1,0,0]\n"
    "v_pk_fma_f32 v[68:69], v[70:71], v[28:29], v[164:165] op_sel_hi:[1,0,1]\n"
    "v_pk_fma_f32 v[68:69], v[70:71], v[28:29], v[68:69] op_sel:[1,1,0] op_sel_hi:[0,1,1] neg_lo:[1,0,0]\n"
    "v_pk_fma_f32 v[70:71], v[68:69], v[28:29], v[166:167] op_sel_hi:[1,0,1]\n"
    "v_pk_fma_f32 v[70:71], v[68:69], v[28:29], v[70:71] op_sel:[1,1,0] op_sel_hi:[0,1,1] neg_lo:[1,0,0]\n"
    "v_pk_fma_f32 v[68:69], v[70:71], v[28:29], v[168:169] op_sel_hi:[1,0,1]\n"
    "v_pk_fma_f32 v[68:69], v[70:71], v[28:29], v[68:69] op_sel:[1,1,0] op_sel_hi:[0,1,1] neg_lo:[1,0,0]\n"
    "v_pk_fma_f32 v[70:71], v[68:69], v[28:29], v[170:171] op_sel_hi:[1,0,1]\n"
    "v_pk_fma_f32 v[70:71], v[68:69], v[28:29], v[70:71] op_sel:[1,1,0] op_sel_hi:[0,1,1] neg_lo:[1,0,0]\n"
    "v_pk_fma_f32 v[68:69], v[70:71], v[28:29], v[172:173] op_sel_hi:[1,0,1]\n"
    "v_pk_fma_f32 v[68:69], v[70:71], v[28:29], v[68:69] op_sel:[1,1,0] op_sel_hi:[0,1,1] neg_lo:[1,0,0]\n"
    "v_pk_fma_f32 v[70:71], v[68:69], v[28:29], v[174:175] op_sel_hi:[1,0,1]\n"
    "v_pk_fma_f32 v[70:71], v[68:69], v[28:29], v[70:71] op_sel:[1,1,0] op_sel_hi:[0,1,1] neg_lo:[1,0,0]\n"
    "v_pk_fma_f32 v[68:69], v[70:71], v[28:29], v[176:177] op_sel_hi:[1,0,1]\n"
    "v_pk_fma_f32 v[68:69], v[70:71], v[28:29], v[68:69] op_sel:[1,1,0] op_sel_hi:[0,1,1] neg_lo:[1,0,0]\n"
    "v_pk_fma_f32 v[70:71], v[68:69], v[28:29], v[178:179] op_sel_hi:[1,0,1]\n"
    "v_pk_fma_f32 v[70:71], v[68:69], v[28:29], v[70:71] op_sel:[1,1,0] op_sel_hi:[0,1,1] neg_lo:[1,0,0]\n"
    "v_pk_fma_f32 v[68:69], v[70:71], v[28:29], v[180:181] op_sel_hi:[1,0,1]\n"
    "v_pk_fma_f32 v[68:69], v[70:71], v[28:29], v[68:69] op_sel:[1,1,0] op_sel_hi:[0,1,1] neg_lo:[1,0,0]\n"
    "v_pk_fma_f32 v[70:71], v[68:69], v[28:29], v[182:183] op_sel_hi:[1,0,1]\n"
    "v_pk_fma_f32 v[70:71], v[68:69], v[28:29], v[70:71] op_sel:[1,1,0] op_sel_hi:[0,1,1] neg_lo:[1,0,0]\n"
    "v_pk_fma_f32 v[68:69], v[70:71], v[28:29], v[184:185] op_sel_hi:[1,0,1]\n"
    "v_pk_fma_f32 v[68:69], v[70:71], v[28:29], v[68:69] op_sel:[1,1,0] op_sel_hi:[0,1,1] neg_lo:[1,0,0]\n"
    "v_pk_fma_f32 v[70:71], v[68:69], v[28:29], v[186:187] op_sel_hi:[1,0,1]\n"
    "v_pk_fma_f32 v[70:71], v[68:69], v[28:29], v[70:71] op_sel:[1,1,0] op_sel_hi:[0,1,1] neg_lo:[1,0,0]\n"
    "v_pk_fma_f32 v[68:69], v[70:71], v[28:29], v[188:189] op_sel_hi:[1,0,1]\n"
    "v_pk_fma_f32 v[68:69], v[70:71], v[28:29], v[68:69] op_sel:[1,1,0] op_sel_hi:[0,1,1] neg_lo:[1,0,0]\n"
    "v_pk_fma_f32 v[70:71], v[68:69], v[28:29], v[190:191] op_sel_hi:[1,0,1]\n"
    "v_pk_fma_f32 v[70:71], v[68:69], v[28:29], v[70:71] op_sel:[1,1,0] op_sel_hi:[0,1,1] neg_lo:[1,0,0]\n"
    "ds_write_b64 v47, v[70:71]\n"
    "v_mfma_f32_32x32x16_bf16 v[192:207], v[228:231], a[20:23], v[192:207]\n"
    "ds_read_b64_tr_b16 v[244:245], v39 offset:8192\n"
    "ds_read_b64_tr_b16 v[246:247], v39 offset:8704\n"
    "s_waitcnt lgkmcnt(2)\n"
    "s_barrier\n"
    "v_mfma_f32_32x32x16_bf16 v[192:207], v[232:235], a[36:39], v[192:207]\n"
    "ds_read_b64_tr_b16 v[224:225], v39 offset:16384\n"
    "ds_read_b64_tr_b16 v[226:227], v39 offset:16896\n"
    "ds_read_b64 v[144:145], v46 offset:0\n"
    "ds_read_b64 v[146:147], v46 offset:256\n"
    "ds_read_b64 v[148:149], v46 offset:512\n"
    "ds_read_b64 v[150:151], v46 offset:768\n"
    "ds_read_b64 v[152:153], v46 offset:1024\n"
    "ds_read_b64 v[154:155], v46 offset:1280\n"
    "ds_read_b64 v[156:157], v46 offset:1536\n"
    "v_mfma_f32_32x32x16_bf16 v[192:207], v[236:239], a[52:55], v[192:207]\n"
    "ds_read_b64_tr_b16 v[228:229], v39 offset:24576\n"
    "ds_read_b64_tr_b16 v[230:231], v39 offset:25088\n"
    "ds_read_b64 v[158:159], v46 offset:1792\n"
    "v_mov_b32 v72, 0\n"
    "v_mov_b32 v73, 0\n"
    "s_waitcnt lgkmcnt(12)\n"
    "v_mfma_f32_32x32x16_bf16 v[192:207], v[240:243], a[8:11], v[192:207]\n"
    "ds_read_b64_tr_b16 v[232:233], v40 offset:0\n"
    "ds_read_b64_tr_b16 v[234:235], v40 offset:512\n"
    "v_mfma_f32_32x32x16_bf16 v[192:207], v[244:247], a[24:27], v[192:207]\n"
    "ds_read_b64_tr_b16 v[236:237], v40 offset:8192\n"
    "ds_read_b64_tr_b16 v[238:239], v40 offset:8704\n"
    "v_cmp_eq_u32 vcc, 1, v215\n"
    "s_waitcnt lgkmcnt(12)\n"
    "v_pk_fma_f32 v[70:71], v[144:145], v[32:33], v[146:147] op_sel_hi:[1,0,1]\n"
    "v_pk_fma_f32 v[70:71], v[144:145], v[32:33], v[70:71] op_sel:[1,1,0] op_sel_hi:[0,1,1] neg_lo:[1,0,0]\n"
    "v_cndmask_b32 v72, v72, v144, vcc\n"
    "v_cndmask_b32 v73, v73, v145, vcc\n"
    "v_cmp_eq_u32 vcc, 2, v215\n"
    "s_waitcnt lgkmcnt(11)\n"
    "v_pk_fma_f32 v[68:69], v[70:71], v[32:33], v[148:149] op_sel_hi:[1,0,1]\n"
    "v_pk_fma_f32 v[68:69], v[70:71], v[32:33], v[68:69] op_sel:[1,1,0] op_sel_hi:[0,1,1] neg_lo:[1,0,0]\n"
    "v_cndmask_b32 v72, v72, v70, vcc\n"
    "v_cndmask_b32 v73, v73, v71, vcc\n"
    "v_cmp_eq_u32 vcc, 3, v215\n"
    "s_waitcnt lgkmcnt(10)\n"
    "v_pk_fma_f32 v[70:71], v[68:69], v[32:33], v[150:151] op_sel_hi:[1,0,1]\n"
    "v_pk_fma_f32 v[70:71], v[68:69], v[32:33], v[70:71] op_sel:[1,1,0] op_sel_hi:[0,1,1] neg_lo:[1,0,0]\n"
    "v_cndmask_b32 v72, v72, v68, vcc\n"
    "v_cndmask_b32 v73, v73, v69, vcc\n"
    "v_cmp_eq_u32 vcc, 4, v215\n"
    "s_waitcnt lgkmcnt(9)\n"
    "v_pk_fma_f32 v[68:69], v[70:71], v[32:33], v[152:153] op_sel_hi:[1,0,1]\n"
    "v_pk_fma_f32 v[68:69], v[70:71], v[32:33], v[68:69] op_sel:[1,1,0] op_sel_hi:[0,1,1] neg_lo:[1,0,0]\n"
    "v_cndmask_b32 v72, v72, v70, vcc\n"
    "v_cndmask_b32 v73, v73, v71, vcc\n"
    "v_cmp_eq_u32 vcc, 5, v215\n"
    "s_waitcnt lgkmcnt(8)\n"
    "v_pk_fma_f32 v[70:71], v[68:69], v[32:33], v[154:155] op_sel_hi:[1,0,1]\n"
    "v_pk_fma_f32 v[70:71], v[68:69], v[32:33], v[70:71] op_sel:[1,1,0] op_sel_hi:[0,1,1] neg_lo:[1,0,0]\n"
    "v_cndmask_b32 v72, v72, v68, vcc\n"
    "v_cndmask_b32 v73, v73, v69, vcc\n"
    "v_cmp_eq_u32 vcc, 6, v215\n"
    "s_waitcnt lgkmcnt(7)\n"
    "v_pk_fma_f32 v[68:69], v[70:71], v[32:33], v[156:157] op_sel_hi:[1,0,1]\n"
    "v_pk_fma_f32 v[68:69], v[70:71], v[32:33], v[68:69] op_sel:[1,1,0] op_sel_hi:[0,1,1] neg_lo:[1,0,0]\n"
    "v_cndmask_b32 v72, v72, v70, vcc\n"
    "v_cndmask_b32 v73, v73, v71, vcc\n"
    "v_cmp_eq_u32 vcc, 7, v215\n"
    "s_waitcnt lgkmcnt(4)\n"
    "v_pk_fma_f32 v[70:71], v[68:69], v[32:33], v[158:159] op_sel_hi:[1,0,1]\n"
    "v_pk_fma_f32 v[70:71], v[68:69], v[32:33], v[70:71] op_sel:[1,1,0] op_sel_hi:[0,1,1] neg_lo:[1,0,0]\n"
    "v_cndmask_b32 v72, v72, v68, vcc\n"
    "v_cndmask_b32 v73, v73, v69, vcc\n"
    "v_cmp_eq_u32 vcc, 0, v8\n"
    "v_mov_b32 v69, s84\n"
    "s_nop 0\n"
    "v_cndmask_b32 v68, v71, v70, vcc\n"
    "buffer_store_dwordx2 v[68:69], v50, s[32:35], s88 offen sc1\n"
    "v_mfma_f32_32x32x16_bf16 v[192:207], v[224:227], a[40:43], v[192:207]\n"
    "ds_read_b64_tr_b16 v[240:241], v40 offset:16384\n"
    "ds_read_b64_tr_b16 v[242:243], v40 offset:16896\n"
    "v_mfma_f32_32x32x16_bf16 v[192:207], v[228:231], a[56:59], v[192:207]\n"
    "ds_read_b64_tr_b16 v[244:245], v40 offset:24576\n"
    "ds_read_b64_tr_b16 v[246:247], v40 offset:25088\n"
    "s_add_u32 m0, s40, 0\n"
    "s_add_u32 s74, s14, 0\n"
    "buffer_load_dwordx4 v15, s[16:19], s74 offen offset:0 lds\n"
    "s_waitcnt lgkmcnt(4)\n"
    "v_mfma_f32_32x32x16_bf16 v[192:207], v[232:235], a[12:15], v[192:207]\n"
    "ds_read_b64_tr_b16 v[112:113], v27 offset:32768\n"
    "ds_read_b64_tr_b16 v[114:115], v27 offset:33280\n"
    "v_mfma_f32_32x32x16_bf16 v[192:207], v[236:239], a[28:31], v[192:207]\n"
    "ds_read_b64_tr_b16 v[116:117], v27 offset:40960\n"
    "ds_read_b64_tr_b16 v[118:119], v27 offset:41472\n"
    "buffer_load_dwordx4 v16, s[16:19], s74 offen offset:1024 lds\n"
    "s_waitcnt lgkmcnt(4)\n"
    "v_mfma_f32_32x32x16_bf16 v[192:207], v[240:243], a[44:47], v[192:207]\n"
    "ds_read_b64_tr_b16 v[120:121], v27 offset:49152\n"
    "ds_read_b64_tr_b16 v[122:123], v27 offset:49664\n"
    "v_mfma_f32_32x32x16_bf16 v[192:207], v[244:247], a[60:63], v[192:207]\n"
    "ds_read_b64_tr_b16 v[124:125], v27 offset:57344\n"
    "ds_read_b64_tr_b16 v[126:127], v27 offset:57856\n"
    "buffer_load_dwordx4 v17, s[16:19], s74 offen offset:2048 lds\n"
    "s_waitcnt lgkmcnt(4)\n"
    "v_mfma_f32_32x32x16_bf16 v[192:207], v[112:115], a[64:67], v[192:207]\n"
    "ds_read_b64_tr_b16 v[224:225], v38 offset:32768\n"
    "ds_read_b64_tr_b16 v[226:227], v38 offset:33280\n"
    "v_mfma_f32_32x32x16_bf16 v[192:207], v[116:119], a[80:83], v[192:207]\n"
    "ds_read_b64_tr_b16 v[228:229], v38 offset:40960\n"
    "ds_read_b64_tr_b16 v[230:231], v38 offset:41472\n"
    "buffer_load_dwordx4 v18, s[16:19], s74 offen offset:3072 lds\n"
    "s_add_u32 m0, s40, 4096\n"
    "s_add_u32 s74, s14, 114688\n"
    "s_waitcnt lgkmcnt(4)\n"
    "v_mfma_f32_32x32x16_bf16 v[192:207], v[120:123], a[96:99], v[192:207]\n"
    "ds_read_b64_tr_b16 v[232:233], v38 offset:49152\n"
    "ds_read_b64_tr_b16 v[234:235], v38 offset:49664\n"
    "v_mfma_f32_32x32x16_bf16 v[192:207], v[124:127], a[112:115], v[192:207]\n"
    "ds_read_b64_tr_b16 v[236:237], v38 offset:57344\n"
    "ds_read_b64_tr_b16 v[238:239], v38 offset:57856\n"
    "buffer_load_dwordx4 v15, s[16:19], s74 offen offset:0 lds\n"
    "s_waitcnt lgkmcnt(4)\n"
    "v_mfma_f32_32x32x16_bf16 v[192:207], v[224:227], a[68:71], v[192:207]\n"
    "ds_read_b64_tr_b16 v[240:241], v39 offset:32768\n"
    "ds_read_b64_tr_b16 v[242:243], v39 offset:33280\n"
    "v_mfma_f32_32x32x16_bf16 v[192:207], v[228:231], a[84:87], v[192:207]\n"
    "ds_read_b64_tr_b16 v[244:245], v39 offset:40960\n"
    "ds_read_b64_tr_b16 v[246:247], v39 offset:41472\n"
    "buffer_load_dwordx4 v16, s[16:19], s74 offen offset:1024 lds\n"
    "s_waitcnt lgkmcnt(4)\n"
    "v_mfma_f32_32x32x16_bf16 v[192:207], v[232:235], a[100:103], v[192:207]\n"
    "ds_read_b64_tr_b16 v[224:225], v39 offset:49152\n"
    "ds_read_b64_tr_b16 v[226:227], v39 offset:49664\n"
    "v_mfma_f32_32x32x16_bf16 v[192:207], v[236:239], a[116:119], v[192:207]\n"
    "ds_read_b64_tr_b16 v[228:229], v39 offset:57344\n"
    "ds_read_b64_tr_b16 v[230:231], v39 offset:57856\n"
    "buffer_load_dwordx4 v17, s[16:19], s74 offen offset:2048 lds\n"
    "v_mov_b32 v53, 0\n"
    "v_mov_b32 v66, 0\n"
    "v_dot2c_f32_bf16 v53, s77, v128\n"
    "v_dot2c_f32_bf16 v66, s77, v129\n"
    "v_dot2c_f32_bf16 v53, s77, v130\n"
    "v_dot2c_f32_bf16 v66, s77, v131\n"
    "v_dot2c_f32_bf16 v53, s77, v132\n"
    "v_dot2c_f32_bf16 v66, s77, v133\n"
    "v_dot2c_f32_bf16 v53, s77, v134\n"
    "v_dot2c_f32_bf16 v66, s77, v135\n"
    "v_dot2c_f32_bf16 v53, s77, v136\n"
    "v_dot2c_f32_bf16 v66, s77, v137\n"
    "v_dot2c_f32_bf16 v53, s77, v138\n"
    "v_dot2c_f32_bf16 v66, s77, v139\n"
    "v_dot2c_f32_bf16 v53, s77, v140\n"
    "v_dot2c_f32_bf16 v66, s77, v141\n"
    "v_dot2c_f32_bf16 v53, s77, v142\n"
    "v_dot2c_f32_bf16 v66, s77, v143\n"
    "v_dot2c_f32_bf16 v53, s77, v112\n"
    "v_dot2c_f32_bf16 v66, s77, v113\n"
    "v_dot2c_f32_bf16 v53, s77, v114\n"
    "v_dot2c_f32_bf16 v66, s77, v115\n"
    "v_dot2c_f32_bf16 v53, s77, v116\n"
    "v_dot2c_f32_bf16 v66, s77, v117\n"
    "v_dot2c_f32_bf16 v53, s77, v118\n"
    "v_dot2c_f32_bf16 v66, s77, v119\n"
    "v_dot2c_f32_bf16 v53, s77, v120\n"
    "v_dot2c_f32_bf16 v66, s77, v121\n"
    "v_dot2c_f32_bf16 v53, s77, v122\n"
    "v_dot2c_f32_bf16 v66, s77, v123\n"
    "v_dot2c_f32_bf16 v53, s77, v124\n"
    "v_dot2c_f32_bf16 v66, s77, v125\n"
    "v_dot2c_f32_bf16 v53, s77, v126\n"
    "v_dot2c_f32_bf16 v66, s77, v127\n"
    "s_waitcnt lgkmcnt(4)\n"
    "v_mfma_f32_32x32x16_bf16 v[192:207], v[240:243], a[72:75], v[192:207]\n"
    "ds_read_b64_tr_b16 v[232:233], v40 offset:32768\n"
    "ds_read_b64_tr_b16 v[234:235], v40 offset:33280\n"
    "v_add_f32 v53, v53, v66\n"
    "ds_write_b32 v45, v53 offset:4096\n"
    "v_mfma_f32_32x32x16_bf16 v[192:207], v[244:247], a[88:91], v[192:207]\n"
    "ds_read_b64_tr_b16 v[236:237], v40 offset:40960\n"
    "ds_read_b64_tr_b16 v[238:239], v40 offset:41472\n"
    "buffer_load_dwordx4 v18, s[16:19], s74 offen offset:3072 lds\n"
    "s_add_u32 m0, s40, 32768\n"
    "s_add_u32 s74, s15, 0\n"
    "s_waitcnt lgkmcnt(5)\n"
    "v_mfma_f32_32x32x16_bf16 v[192:207], v[224:227], a[104:107], v[192:207]\n"
    "ds_read_b64_tr_b16 v[240:241], v40 offset:49152\n"
    "ds_read_b64_tr_b16 v[242:243], v40 offset:49664\n"
    "v_mfma_f32_32x32x16_bf16 v[192:207], v[228:231], a[120:123], v[192:207]\n"
    "ds_read_b64_tr_b16 v[244:245], v40 offset:57344\n"
    "ds_read_b64_tr_b16 v[246:247], v40 offset:57856\n"
    "buffer_load_dwordx4 v15, s[16:19], s74 offen offset:0 lds\n"
    "v_xor_b32 v27, 64, v27\n"
    "v_xor_b32 v38, 64, v38\n"
    "v_xor_b32 v39, 64, v39\n"
    "v_xor_b32 v40, 64, v40\n"
    "s_waitcnt lgkmcnt(6)\n"
    "s_barrier\n"
    "s_waitcnt lgkmcnt(4)\n"
    "v_mfma_f32_32x32x16_bf16 v[192:207], v[232:235], a[76:79], v[192:207]\n"
    "ds_read_b64_tr_b16 v[128:129], v27 offset:0\n"
    "ds_read_b64_tr_b16 v[130:131], v27 offset:512\n"
    "ds_read_b128 v[248:251], v43 offset:4096\n"
    "s_waitcnt lgkmcnt(0)\n"
    "v_add_f32 v248, v248, v249\n"
    "v_add_f32 v250, v250, v251\n"
    "v_mfma_f32_32x32x16_bf16 v[192:207], v[236:239], a[92:95], v[192:207]\n"
    "ds_read_b64_tr_b16 v[132:133], v27 offset:8192\n"
    "ds_read_b64_tr_b16 v[134:135], v27 offset:8704\n"
    "buffer_load_dwordx4 v16, s[16:19], s74 offen offset:1024 lds\n"
    "v_add_f32 v248, v248, v250\n"
    "v_and_b32 v249, 0xffff0000, v248\n"
    "v_sub_f32 v249, v248, v249\n"
    "v_mfma_f32_32x32x16_bf16 v[192:207], v[240:243], a[108:111], v[192:207]\n"
    "ds_read_b64_tr_b16 v[136:137], v27 offset:16384\n"
    "ds_read_b64_tr_b16 v[138:139], v27 offset:16896\n"
    "v_and_b32 v250, 0xffff0000, v249\n"
    "v_perm_b32 v60, v249, v248, s29\n"
    "v_sub_f32 v250, v249, v250\n"
    "v_mfma_f32_32x32x16_bf16 v[192:207], v[244:247], a[124:127], v[192:207]\n"
    "ds_read_b64_tr_b16 v[140:141], v27 offset:24576\n"
    "ds_read_b64_tr_b16 v[142:143], v27 offset:25088\n"
    "buffer_load_dwordx4 v17, s[16:19], s74 offen offset:2048 lds\n"
    "v_perm_b32 v61, v11, v250, s30\n"
    "s_nop 1\n"
    "v_mfma_f32_32x32x16_bf16 v[96:111], v[60:63], v[56:59], v[192:207]\n"
    "s_waitcnt lgkmcnt(4)\n"
    "v_mfma_f32_32x32x16_bf16 v[192:207], v[128:131], a[0:3], 0\n"
    "ds_read_b64_tr_b16 v[224:225], v38 offset:0\n"
    "ds_read_b64_tr_b16 v[226:227], v38 offset:512\n"
    "s_add_u32 s66, s78, 0\n"
    "s_lshl_b32 s66, s66, 11\n"
    "s_add_u32 s66, s66, s28\n"
    "v_mfma_f32_32x32x16_bf16 v[192:207], v[132:135], a[16:19], v[192:207]\n"
    "ds_read_b64_tr_b16 v[228:229], v38 offset:8192\n"
    "ds_read_b64_tr_b16 v[230:231], v38 offset:8704\n"
    "buffer_load_dwordx4 v18, s[16:19], s74 offen offset:3072 lds\n"
    "s_waitcnt lgkmcnt(4)\n"
    "v_mfma_f32_32x32x16_bf16 v[192:207], v[136:139], a[32:35], v[192:207]\n"
    "ds_read_b64_tr_b16 v[232:233], v38 offset:16384\n"
    "ds_read_b64_tr_b16 v[234:235], v38 offset:16896\n"
    "v_mfma_f32_32x32x16_bf16 v[192:207], v[140:143], a[48:51], v[192:207]\n"
    "ds_read_b64_tr_b16 v[236:237], v38 offset:24576\n"
    "ds_read_b64_tr_b16 v[238:239], v38 offset:25088\n"
    "buffer_load_dwordx4 v[216:219], v51, s[32:35], 0 offen sc1\n"
    "v_mul_f32 v96, v64, v96\n"
    "v_mul_f32 v97, v64, v97\n"
    "v_max_f32 v96, 0, v96\n"
    "s_waitcnt lgkmcnt(4)\n"
    "v_mfma_f32_32x32x16_bf16 v[192:207], v[224:227], a[4:7], v[192:207]\n"
    "ds_read_b64_tr_b16 v[240:241], v39 offset:0\n"
    "ds_read_b64_tr_b16 v[242:243], v39 offset:512\n"
    "v_max_f32 v97, 0, v97\n"
    "v_mul_f32 v96, v96, v96\n"
    "v_mul_f32 v97, v97, v97\n"
    "v_mfma_f32_32x32x16_bf16 v[192:207], v[228:231], a[20:23], v[192:207]\n"
    "ds_read_b64_tr_b16 v[244:245], v39 offset:8192\n"
    "ds_read_b64_tr_b16 v[246:247], v39 offset:8704\n"
    "buffer_load_dwordx4 v[220:223], v51, s[32:35], s87 offen sc1\n"
    "s_add_u32 m0, s40, 36864\n"
    "s_add_u32 s74, s15, 114688\n"
    "v_cvt_pk_bf16_f32 v80, v96, v97\n"
    "s_waitcnt lgkmcnt(4)\n"
    "v_mfma_f32_32x32x16_bf16 v[192:207], v[232:235], a[36:39], v[192:207]\n"
    "ds_read_b64_tr_b16 v[224:225], v39 offset:16384\n"
    "ds_read_b64_tr_b16 v[226:227], v39 offset:16896\n"
    "v_mul_f32 v98, v64, v98\n"
    "v_mul_f32 v99, v64, v99\n"
    "v_max_f32 v98, 0, v98\n"
    "v_mfma_f32_32x32x16_bf16 v[192:207], v[236:239], a[52:55], v[192:207]\n"
    "ds_read_b64_tr_b16 v[228:229], v39 offset:24576\n"
    "ds_read_b64_tr_b16 v[230:231], v39 offset:25088\n"
    "buffer_load_dwordx4 v15, s[16:19], s74 offen offset:0 lds\n"
    "v_max_f32 v99, 0, v99\n"
    "v_mul_f32 v98, v98, v98\n"
    "v_mul_f32 v99, v99, v99\n"
    "s_waitcnt lgkmcnt(4)\n"
    "v_mfma_f32_32x32x16_bf16 v[192:207], v[240:243], a[8:11], v[192:207]\n"
    "ds_read_b64_tr_b16 v[232:233], v40 offset:0\n"
    "ds_read_b64_tr_b16 v[234:235], v40 offset:512\n"
    "v_cvt_pk_bf16_f32 v81, v98, v99\n"
    "v_mul_f32 v100, v64, v100\n"
    "v_mul_f32 v101, v64, v101\n"
    "v_mfma_f32_32x32x16_bf16 v[192:207], v[244:247], a[24:27], v[192:207]\n"
    "ds_read_b64_tr_b16 v[236:237], v40 offset:8192\n"
    "ds_read_b64_tr_b16 v[238:239], v40 offset:8704\n"
    "buffer_load_dwordx4 v16, s[16:19], s74 offen offset:1024 lds\n"
    "v_max_f32 v100, 0, v100\n"
    "v_max_f32 v101, 0, v101\n"
    "v_mul_f32 v100, v100, v100\n"
    "s_waitcnt lgkmcnt(4)\n"
    "v_mfma_f32_32x32x16_bf16 v[192:207], v[224:227], a[40:43], v[192:207]\n"
    "ds_read_b64_tr_b16 v[240:241], v40 offset:16384\n"
    "ds_read_b64_tr_b16 v[242:243], v40 offset:16896\n"
    "v_mul_f32 v101, v101, v101\n"
    "v_cvt_pk_bf16_f32 v82, v100, v101\n"
    "v_mul_f32 v102, v64, v102\n"
    "v_mfma_f32_32x32x16_bf16 v[192:207], v[228:231], a[56:59], v[192:207]\n"
    "ds_read_b64_tr_b16 v[244:245], v40 offset:24576\n"
    "ds_read_b64_tr_b16 v[246:247], v40 offset:25088\n"
    "buffer_load_dwordx4 v17, s[16:19], s74 offen offset:2048 lds\n"
    "v_mul_f32 v103, v64, v103\n"
    "v_max_f32 v102, 0, v102\n"
    "v_max_f32 v103, 0, v103\n"
    "s_waitcnt lgkmcnt(4)\n"
    "v_mfma_f32_32x32x16_bf16 v[192:207], v[232:235], a[12:15], v[192:207]\n"
    "ds_read_b64_tr_b16 v[112:113], v27 offset:32768\n"
    "ds_read_b64_tr_b16 v[114:115], v27 offset:33280\n"
    "v_mul_f32 v102, v102, v102\n"
    "v_mul_f32 v103, v103, v103\n"
    "v_cvt_pk_bf16_f32 v83, v102, v103\n"
    ".Lck0:\n"
    "s_waitcnt vmcnt(4)\n"
    "v_cmp_eq_u32 vcc, s84, v217\n"
    "v_cmp_eq_u32 s[56:57], s84, v219\n"
    "v_cmp_le_i32 s[58:59], 0, v214\n"
    "s_and_b64 vcc, vcc, s[56:57]\n"
    "s_nop 0\n"
    "v_cndmask_b32 v216, v216, 0, s[58:59]\n"
    "v_cndmask_b32 v217, v218, 0, s[58:59]\n"
    "s_or_b64 s[2:3], vcc, s[58:59]\n"
    "s_waitcnt vmcnt(3)\n"
    "v_cmp_eq_u32 vcc, s84, v221\n"
    "v_cmp_eq_u32 s[56:57], s84, v223\n"
    "v_cmp_le_i32 s[58:59], -1, v214\n"
    "s_and_b64 vcc, vcc, s[56:57]\n"
    "s_nop 0\n"
    "v_cndmask_b32 v220, v220, 0, s[58:59]\n"
    "v_cndmask_b32 v221, v222, 0, s[58:59]\n"
    "s_or_b64 vcc, vcc, s[58:59]\n"
    "s_and_b64 vcc, vcc, s[2:3]\n"
    "s_cmp_eq_u64 vcc, -1\n"
    "s_cbranch_scc0 .Lsl0\n"
    ".Lok0:\n"
    "ds_write_b64 v212, v[216:217]\n"
    "ds_write_b64 v213, v[220:221]\n"
    "v_mfma_f32_32x32x16_bf16 v[192:207], v[236:239], a[28:31], v[192:207]\n"
    "ds_read_b64_tr_b16 v[116:117], v27 offset:40960\n"
    "ds_read_b64_tr_b16 v[118:119], v27 offset:41472\n"
    "buffer_load_dwordx4 v18, s[16:19], s74 offen offset:3072 lds\n"
    "s_add_u32 m0, s72, 65536\n"
    "buffer_store_dwordx4 v[80:83], v42, s[32:35], s66 offen offset:0 sc1\n"
    "v_mul_f32 v104, v64, v104\n"
    "s_waitcnt lgkmcnt(6)\n"
    "v_mfma_f32_32x32x16_bf16 v[192:207], v[240:243], a[44:47], v[192:207]\n"
    "ds_read_b64_tr_b16 v[120:121], v27 offset:49152\n"
    "ds_read_b64_tr_b16 v[122:123], v27 offset:49664\n"
    "v_mul_f32 v105, v64, v105\n"
    "v_max_f32 v104, 0, v104\n"
    "v_max_f32 v105, 0, v105\n"
    "v_mul_f32 v104, v104, v104\n"
    "v_mul_f32 v105, v105, v105\n"
    "v_cvt_pk_bf16_f32 v84, v104, v105\n"
    "v_mul_f32 v106, v64, v106\n"
    "v_mul_f32 v107, v64, v107\n"
    "v_max_f32 v106, 0, v106\n"
    "v_max_f32 v107, 0, v107\n"
    "v_mul_f32 v106, v106, v106\n"
    "v_mul_f32 v107, v107, v107\n"
    "v_cvt_pk_bf16_f32 v85, v106, v107\n"
    "v_mul_f32 v108, v64, v108\n"
    "v_mul_f32 v109, v64, v109\n"
    "v_max_f32 v108, 0, v108\n"
    "v_max_f32 v109, 0, v109\n"
    "v_mul_f32 v108, v108, v108\n"
    "v_mul_f32 v109, v109, v109\n"
    "v_cvt_pk_bf16_f32 v86, v108, v109\n"
    "v_mul_f32 v110, v64, v110\n"
    "v_mul_f32 v111, v64, v111\n"
    "v_max_f32 v110, 0, v110\n"
    "v_max_f32 v111, 0, v111\n"
    "v_mul_f32 v110, v110, v110\n"
    "v_mul_f32 v111, v111, v111\n"
    "v_cvt_pk_bf16_f32 v87, v110, v111\n"
    "buffer_store_dwordx4 v[84:87], v42, s[32:35], s66 offen offset:1024 sc1\n"
    "s_waitcnt lgkmcnt(4)\n"
    "s_barrier\n"
    "v_mfma_f32_32x32x16_bf16 v[192:207], v[244:247], a[60:63], v[192:207]\n"
    "ds_read_b64_tr_b16 v[124:125], v27 offset:57344\n"
    "ds_read_b64_tr_b16 v[126:127], v27 offset:57856\n"
    "buffer_load_dwordx4 v19, s[20:23], s48 offen offset:0 lds\n"
    "v_add_u32 v3, 4294962176, v46\n"
    "ds_read_b64 v[144:145], v3 offset:0\n"
    "ds_read_b64 v[146:147], v3 offset:256\n"
    "s_waitcnt lgkmcnt(6)\n"
    "v_mfma_f32_32x32x16_bf16 v[192:207], v[112:115], a[64:67], v[192:207]\n"
    "ds_read_b64_tr_b16 v[224:225], v38 offset:32768\n"
    "ds_read_b64_tr_b16 v[226:227], v38 offset:33280\n"
    "ds_read_b64 v[148:149], v3 offset:512\n"
    "ds_read_b64 v[150:151], v3 offset:768\n"
    "ds_read_b64 v[152:153], v3 offset:1024\n"
    "ds_read_b64 v[154:155], v3 offset:1280\n"
    "v_mfma_f32_32x32x16_bf16 v[192:207], v[116:119], a[80:83], v[192:207]\n"
    "ds_read_b64_tr_b16 v[228:229], v38 offset:40960\n"
    "ds_read_b64_tr_b16 v[230:231], v38 offset:41472\n"
    "buffer_load_dwordx4 v20, s[20:23], s48 offen offset:1024 lds\n"
    "ds_read_b64 v[156:157], v3 offset:1536\n"
    "ds_read_b64 v[158:159], v3 offset:1792\n"
    "ds_read_b64 v[216:217], v3 offset:2048\n"
    "ds_read_b64 v[218:219], v3 offset:2304\n"
    "s_waitcnt lgkmcnt(14)\n"
    "v_mfma_f32_32x32x16_bf16 v[192:207], v[120:123], a[96:99], v[192:207]\n"
    "ds_read_b64_tr_b16 v[232:233], v38 offset:49152\n"
    "ds_read_b64_tr_b16 v[234:235], v38 offset:49664\n"
    "ds_read_b64 v[220:221], v3 offset:2560\n"
    "ds_read_b64 v[222:223], v3 offset:2816\n"
    "ds_read_b64 v[250:251], v3 offset:3072\n"
    "ds_read_b64 v[252:253], v3 offset:3328\n"
    "v_mfma_f32_32x32x16_bf16 v[192:207], v[124:127], a[112:115], v[192:207]\n"
    "ds_read_b64_tr_b16 v[236:237], v38 offset:57344\n"
    "ds_read_b64_tr_b16 v[238:239], v38 offset:57856\n"
    "buffer_load_dwordx4 v21, s[20:23], s48 offen offset:2048 lds\n"
    "ds_read_b64 v[254:255], v3 offset:3584\n"
    "s_waitcnt lgkmcnt(15)\n"
    "v_pk_fma_f32 v[70:71], v[144:145], v[34:35], v[146:147] op_sel_hi:[1,0,1]\n"
    "v_pk_fma_f32 v[70:71], v[144:145], v[34:35], v[70:71] op_sel:[1,1,0] op_sel_hi:[0,1,1] neg_lo:[1,0,0]\n"
    "s_waitcnt lgkmcnt(15)\n"
    "v_pk_fma_f32 v[68:69], v[70:71], v[34:35], v[148:149] op_sel_hi:[1,0,1]\n"
    "v_pk_fma_f32 v[68:69], v[70:71], v[34:35], v[68:69] op_sel:[1,1,0] op_sel_hi:[0,1,1] neg_lo:[1,0,0]\n"
    "s_waitcnt lgkmcnt(15)\n"
    "v_pk_fma_f32 v[70:71], v[68:69], v[34:35], v[150:151] op_sel_hi:[1,0,1]\n"
    "v_pk_fma_f32 v[70:71], v[68:69], v[34:35], v[70:71] op_sel:[1,1,0] op_sel_hi:[0,1,1] neg_lo:[1,0,0]\n"
    "s_waitcnt lgkmcnt(15)\n"
    "v_pk_fma_f32 v[68:69], v[70:71], v[34:35], v[152:153] op_sel_hi:[1,0,1]\n"
    "v_pk_fma_f32 v[68:69], v[70:71], v[34:35], v[68:69] op_sel:[1,1,0] op_sel_hi:[0,1,1] neg_lo:[1,0,0]\n"
    "s_waitcnt lgkmcnt(15)\n"
    "v_pk_fma_f32 v[70:71], v[68:69], v[34:35], v[154:155] op_sel_hi:[1,0,1]\n"
    "v_pk_fma_f32 v[70:71], v[68:69], v[34:35], v[70:71] op_sel:[1,1,0] op_sel_hi:[0,1,1] neg_lo:[1,0,0]\n"
    "s_waitcnt lgkmcnt(12)\n"
    "v_pk_fma_f32 v[68:69], v[70:71], v[34:35], v[156:157] op_sel_hi:[1,0,1]\n"
    "v_pk_fma_f32 v[68:69], v[70:71], v[34:35], v[68:69] op_sel:[1,1,0] op_sel_hi:[0,1,1] neg_lo:[1,0,0]\n"
    "s_waitcnt lgkmcnt(11)\n"
    "v_pk_fma_f32 v[70:71], v[68:69], v[34:35], v[158:159] op_sel_hi:[1,0,1]\n"
    "v_pk_fma_f32 v[70:71], v[68:69], v[34:35], v[70:71] op_sel:[1,1,0] op_sel_hi:[0,1,1] neg_lo:[1,0,0]\n"
    "s_waitcnt lgkmcnt(10)\n"
    "v_pk_fma_f32 v[68:69], v[70:71], v[34:35], v[216:217] op_sel_hi:[1,0,1]\n"
    "v_pk_fma_f32 v[68:69], v[70:71], v[34:35], v[68:69] op_sel:[1,1,0] op_sel_hi:[0,1,1] neg_lo:[1,0,0]\n"
    "s_waitcnt lgkmcnt(9)\n"
    "v_pk_fma_f32 v[70:71], v[68:69], v[34:35], v[218:219] op_sel_hi:[1,0,1]\n"
    "v_pk_fma_f32 v[70:71], v[68:69], v[34:35], v[70:71] op_sel:[1,1,0] op_sel_hi:[0,1,1] neg_lo:[1,0,0]\n"
    "s_waitcnt lgkmcnt(6)\n"
    "v_pk_fma_f32 v[68:69], v[70:71], v[34:35], v[220:221] op_sel_hi:[1,0,1]\n"
    "v_pk_fma_f32 v[68:69], v[70:71], v[34:35], v[68:69] op_sel:[1,1,0] op_sel_hi:[0,1,1] neg_lo:[1,0,0]\n"
    "s_waitcnt lgkmcnt(5)\n"
    "v_pk_fma_f32 v[70:71], v[68:69], v[34:35], v[222:223] op_sel_hi:[1,0,1]\n"
    "v_pk_fma_f32 v[70:71], v[68:69], v[34:35], v[70:71] op_sel:[1,1,0] op_sel_hi:[0,1,1] neg_lo:[1,0,0]\n"
    "s_waitcnt lgkmcnt(4)\n"
    "v_pk_fma_f32 v[68:69], v[70:71], v[34:35], v[250:251] op_sel_hi:[1,0,1]\n"
    "v_pk_fma_f32 v[68:69], v[70:71], v[34:35], v[68:69] op_sel:[1,1,0] op_sel_hi:[0,1,1] neg_lo:[1,0,0]\n"
    "s_waitcnt lgkmcnt(3)\n"
    "v_pk_fma_f32 v[70:71], v[68:69], v[34:35], v[252:253] op_sel_hi:[1,0,1]\n"
    "v_pk_fma_f32 v[70:71], v[68:69], v[34:35], v[70:71] op_sel:[1,1,0] op_sel_hi:[0,1,1] neg_lo:[1,0,0]\n"
    "s_waitcnt lgkmcnt(0)\n"
    "v_pk_fma_f32 v[68:69], v[70:71], v[34:35], v[254:255] op_sel_hi:[1,0,1]\n"
    "v_pk_fma_f32 v[68:69], v[70:71], v[34:35], v[68:69] op_sel:[1,1,0] op_sel_hi:[0,1,1] neg_lo:[1,0,0]\n"
    "v_pk_fma_f32 v[72:73], v[68:69], v[36:37], v[72:73] op_sel_hi:[1,0,1]\n"
    "v_pk_fma_f32 v[72:73], v[68:69], v[36:37], v[72:73] op_sel:[1,1,0] op_sel_hi:[0,1,1] neg_lo:[1,0,0]\n"
    "v_pk_fma_f32 v[160:161], v[72:73], v[28:29], v[160:161] op_sel_hi:[1,0,1]\n"
    "v_pk_fma_f32 v[160:161], v[72:73], v[28:29], v[160:161] op_sel:[1,1,0] op_sel_hi:[0,1,1] neg_lo:[1,0,0]\n"
    "v_pk_fma_f32 v[162:163], v[160:161], v[28:29], v[162:163] op_sel_hi:[1,0,1]\n"
    "v_pk_fma_f32 v[162:163], v[160:161], v[28:29], v[162:163] op_sel:[1,1,0] op_sel_hi:[0,1,1] neg_lo:[1,0,0]\n"
    "v_pk_fma_f32 v[164:165], v[162:163], v[28:29], v[164:165] op_sel_hi:[1,0,1]\n"
    "v_pk_fma_f32 v[164:165], v[162:163], v[28:29], v[164:165] op_sel:[1,1,0] op_sel_hi:[0,1,1] neg_lo:[1,0,0]\n"
    "v_pk_fma_f32 v[166:167], v[164:165], v[28:29], v[166:167] op_sel_hi:[1,0,1]\n"
    "v_pk_fma_f32 v[166:167], v[164:165], v[28:29], v[166:167] op_sel:[1,1,0] op_sel_hi:[0,1,1] neg_lo:[1,0,0]\n"
    "v_pk_fma_f32 v[168:169], v[166:167], v[28:29], v[168:169] op_sel_hi:[1,0,1]\n"
    "v_pk_fma_f32 v[168:169], v[166:167], v[28:29], v[168:169] op_sel:[1,1,0] op_sel_hi:[0,1,1] neg_lo:[1,0,0]\n"
    "v_pk_fma_f32 v[170:171], v[168:169], v[28:29], v[170:171] op_sel_hi:[1,0,1]\n"
    "v_pk_fma_f32 v[170:171], v[168:169], v[28:29], v[170:171] op_sel:[1,1,0] op_sel_hi:[0,1,1] neg_lo:[1,0,0]\n"
    "v_pk_fma_f32 v[172:173], v[170:171], v[28:29], v[172:173] op_sel_hi:[1,0,1]\n"
    "v_pk_fma_f32 v[172:173], v[170:171], v[28:29], v[172:173] op_sel:[1,1,0] op_sel_hi:[0,1,1] neg_lo:[1,0,0]\n"
    "v_pk_fma_f32 v[174:175], v[172:173], v[28:29], v[174:175] op_sel_hi:[1,0,1]\n"
    "v_pk_fma_f32 v[174:175], v[172:173], v[28:29], v[174:175] op_sel:[1,1,0] op_sel_hi:[0,1,1] neg_lo:[1,0,0]\n"
    "v_pk_fma_f32 v[176:177], v[174:175], v[28:29], v[176:177] op_sel_hi:[1,0,1]\n"
    "v_pk_fma_f32 v[176:177], v[174:175], v[28:29], v[176:177] op_sel:[1,1,0] op_sel_hi:[0,1,1] neg_lo:[1,0,0]\n"
    "v_pk_fma_f32 v[178:179], v[176:177], v[28:29], v[178:179] op_sel_hi:[1,0,1]\n"
    "v_pk_fma_f32 v[178:179], v[176:177], v[28:29], v[178:179] op_sel:[1,1,0] op_sel_hi:[0,1,1] neg_lo:[1,0,0]\n"
    "v_pk_fma_f32 v[180:181], v[178:179], v[28:29], v[180:181] op_sel_hi:[1,0,1]\n"
    "v_pk_fma_f32 v[180:181], v[178:179], v[28:29], v[180:181] op_sel:[1,1,0] op_sel_hi:[0,1,1] neg_lo:[1,0,0]\n"
    "v_pk_fma_f32 v[182:183], v[180:181], v[28:29], v[182:183] op_sel_hi:[1,0,1]\n"
    "v_pk_fma_f32 v[182:183], v[180:181], v[28:29], v[182:183] op_sel:[1,1,0] op_sel_hi:[0,1,1] neg_lo:[1,0,0]\n"
    "v_pk_fma_f32 v[184:185], v[182:183], v[28:29], v[184:185] op_sel_hi:[1,0,1]\n"
    "v_pk_fma_f32 v[184:185], v[182:183], v[28:29], v[184:185] op_sel:[1,1,0] op_sel_hi:[0,1,1] neg_lo:[1,0,0]\n"
    "v_pk_fma_f32 v[186:187], v[184:185], v[28:29], v[186:187] op_sel_hi:[1,0,1]\n"
    "v_pk_fma_f32 v[186:187], v[184:185], v[28:29], v[186:187] op_sel:[1,1,0] op_sel_hi:[0,1,1] neg_lo:[1,0,0]\n"
    "v_pk_fma_f32 v[188:189], v[186:187], v[28:29], v[188:189] op_sel_hi:[1,0,1]\n"
    "v_pk_fma_f32 v[188:189], v[186:187], v[28:29], v[188:189] op_sel:[1,1,0] op_sel_hi:[0,1,1] neg_lo:[1,0,0]\n"
    "v_pk_fma_f32 v[190:191], v[188:189], v[28:29], v[190:191] op_sel_hi:[1,0,1]\n"
    "v_pk_fma_f32 v[190:191], v[188:189], v[28:29], v[190:191] op_sel:[1,1,0] op_sel_hi:[0,1,1] neg_lo:[1,0,0]\n"
    "v_pk_mul_f32 v[68:69], v[160:161], v[30:31] op_sel_hi:[1,0] neg_hi:[1,0]\n"
    "v_pk_fma_f32 v[68:69], v[160:161], v[30:31], v[68:69] op_sel:[1,1,0] op_sel_hi:[0,1,1] neg_lo:[1,0,0] neg_hi:[1,0,0]\n"
    "v_pk_mul_f32 v[160:161], v[162:163], v[30:31] op_sel_hi:[1,0] neg_hi:[1,0]\n"
    "v_pk_fma_f32 v[160:161], v[162:163], v[30:31], v[160:161] op_sel:[1,1,0] op_sel_hi:[0,1,1] neg_lo:[1,0,0] neg_hi:[1,0,0]\n"
    "v_pk_mul_f32 v[162:163], v[164:165], v[30:31] op_sel_hi:[1,0] neg_hi:[1,0]\n"
    "v_pk_fma_f32 v[162:163], v[164:165], v[30:31], v[162:163] op_sel:[1,1,0] op_sel_hi:[0,1,1] neg_lo:[1,0,0] neg_hi:[1,0,0]\n"
    "v_pk_mul_f32 v[164:165], v[166:167], v[30:31] op_sel_hi:[1,0] neg_hi:[1,0]\n"
    "v_pk_fma_f32 v[164:165], v[166:167], v[30:31], v[164:165] op_sel:[1,1,0] op_sel_hi:[0,1,1] neg_lo:[1,0,0] neg_hi:[1,0,0]\n"
    "v_pk_mul_f32 v[166:167], v[168:169], v[30:31] op_sel_hi:[1,0] neg_hi:[1,0]\n"
    "v_pk_fma_f32 v[166:167], v[168:169], v[30:31], v[166:167] op_sel:[1,1,0] op_sel_hi:[0,1,1] neg_lo:[1,0,0] neg_hi:[1,0,0]\n"
    "v_pk_mul_f32 v[168:169], v[170:171], v[30:31] op_sel_hi:[1,0] neg_hi:[1,0]\n"
    "v_pk_fma_f32 v[168:169], v[170:171], v[30:31], v[168:169] op_sel:[1,1,0] op_sel_hi:[0,1,1] neg_lo:[1,0,0] neg_hi:[1,0,0]\n"
    "v_pk_mul_f32 v[170:171], v[172:173], v[30:31] op_sel_hi:[1,0] neg_hi:[1,0]\n"
    "v_pk_fma_f32 v[170:171], v[172:173], v[30:31], v[170:171] op_sel:[1,1,0] op_sel_hi:[0,1,1] neg_lo:[1,0,0] neg_hi:[1,0,0]\n"
    "v_pk_mul_f32 v[172:173], v[174:175], v[30:31] op_sel_hi:[1,0] neg_hi:[1,0]\n"
    "v_pk_fma_f32 v[172:173], v[174:175], v[30:31], v[172:173] op_sel:[1,1,0] op_sel_hi:[0,1,1] neg_lo:[1,0,0] neg_hi:[1,0,0]\n"
    "v_pk_mul_f32 v[174:175], v[176:177], v[30:31] op_sel_hi:[1,0] neg_hi:[1,0]\n"
    "v_pk_fma_f32 v[174:175], v[176:177], v[30:31], v[174:175] op_sel:[1,1,0] op_sel_hi:[0,1,1] neg_lo:[1,0,0] neg_hi:[1,0,0]\n"
    "v_pk_mul_f32 v[176:177], v[178:179], v[30:31] op_sel_hi:[1,0] neg_hi:[1,0]\n"
    "v_pk_fma_f32 v[176:177], v[178:179], v[30:31], v[176:177] op_sel:[1,1,0] op_sel_hi:[0,1,1] neg_lo:[1,0,0] neg_hi:[1,0,0]\n"
    "v_pk_mul_f32 v[178:179], v[180:181], v[30:31] op_sel_hi:[1,0] neg_hi:[1,0]\n"
    "v_pk_fma_f32 v[178:179], v[180:181], v[30:31], v[178:179] op_sel:[1,1,0] op_sel_hi:[0,1,1] neg_lo:[1,0,0] neg_hi:[1,0,0]\n"
    "v_pk_mul_f32 v[180:181], v[182:183], v[30:31] op_sel_hi:[1,0] neg_hi:[1,0]\n"
    "v_pk_fma_f32 v[180:181], v[182:183], v[30:31], v[180:181] op_sel:[1,1,0] op_sel_hi:[0,1,1] neg_lo:[1,0,0] neg_hi:[1,0,0]\n"
    "v_pk_mul_f32 v[182:183], v[184:185], v[30:31] op_sel_hi:[1,0] neg_hi:[1,0]\n"
    "v_pk_fma_f32 v[182:183], v[184:185], v[30:31], v[182:183] op_sel:[1,1,0] op_sel_hi:[0,1,1] neg_lo:[1,0,0] neg_hi:[1,0,0]\n"
    "v_pk_mul_f32 v[184:185], v[186:187], v[30:31] op_sel_hi:[1,0] neg_hi:[1,0]\n"
    "v_pk_fma_f32 v[184:185], v[186:187], v[30:31], v[184:185] op_sel:[1,1,0] op_sel_hi:[0,1,1] neg_lo:[1,0,0] neg_hi:[1,0,0]\n"
    "v_pk_mul_f32 v[186:187], v[188:189], v[30:31] op_sel_hi:[1,0] neg_hi:[1,0]\n"
    "v_pk_fma_f32 v[186:187], v[188:189], v[30:31], v[186:187] op_sel:[1,1,0] op_sel_hi:[0,1,1] neg_lo:[1,0,0] neg_hi:[1,0,0]\n"
    "v_pk_mul_f32 v[188:189], v[190:191], v[30:31] op_sel_hi:[1,0] neg_hi:[1,0]\n"
    "v_pk_fma_f32 v[188:189], v[190:191], v[30:31], v[188:189] op_sel:[1,1,0] op_sel_hi:[0,1,1] neg_lo:[1,0,0] neg_hi:[1,0,0]\n"
    "v_mfma_f32_32x32x16_bf16 v[192:207], v[224:227], a[68:71], v[192:207]\n"
    "ds_read_b64_tr_b16 v[240:241], v39 offset:32768\n"
    "ds_read_b64_tr_b16 v[242:243], v39 offset:33280\n"
    "v_cvt_pk_bf16_f32 v144, v68, v160\n"
    "v_cvt_pk_bf16_f32 v145, v162, v164\n"
    "v_cvt_pk_bf16_f32 v146, v166, v168\n"
    "v_cvt_pk_bf16_f32 v147, v170, v172\n"
    "v_mfma_f32_32x32x16_bf16 v[192:207], v[228:231], a[84:87], v[192:207]\n"
    "ds_read_b64_tr_b16 v[244:245], v39 offset:40960\n"
    "ds_read_b64_tr_b16 v[246:247], v39 offset:41472\n"
    "buffer_load_dwordx4 v22, s[20:23], s48 offen offset:3072 lds\n"
    "ds_write_b128 v208, v[144:147]\n"
    "v_cvt_pk_bf16_f32 v148, v174, v176\n"
    "v_cvt_pk_bf16_f32 v149, v178, v180\n"
    "v_cvt_pk_bf16_f32 v150, v182, v184\n"
    "v_mfma_f32_32x32x16_bf16 v[192:207], v[232:235], a[100:103], v[192:207]\n"
    "ds_read_b64_tr_b16 v[224:225], v39 offset:49152\n"
    "ds_read_b64_tr_b16 v[226:227], v39 offset:49664\n"
    "v_cvt_pk_bf16_f32 v151, v186, v188\n"
    "ds_write_b128 v209, v[148:151]\n"
    "ds_read_b64_tr_b16 a[176:177], v210 offset:0\n"
    "ds_read_b64_tr_b16 a[178:179], v211 offset:256\n"
    "v_mfma_f32_32x32x16_bf16 v[192:207], v[236:239], a[116:119], v[192:207]\n"
    "ds_read_b64_tr_b16 v[228:229], v39 offset:57344\n"
    "ds_read_b64_tr_b16 v[230:231], v39 offset:57856\n"
    "ds_read_b64_tr_b16 a[180:181], v210 offset:1024\n"
    "ds_read_b64_tr_b16 a[182:183], v211 offset:1280\n"
    "v_cvt_pk_bf16_f32 v152, v69, v161\n"
    "v_cvt_pk_bf16_f32 v153, v163, v165\n"
    "v_cvt_pk_bf16_f32 v154, v167, v169\n"
    "v_mov_b32 v53, 0\n"
    "v_mov_b32 v66, 0\n"
    "v_dot2c_f32_bf16 v53, s77, v128\n"
    "v_dot2c_f32_bf16 v66, s77, v129\n"
    "v_dot2c_f32_bf16 v53, s77, v130\n"
    "v_dot2c_f32_bf16 v66, s77, v131\n"
    "v_dot2c_f32_bf16 v53, s77, v132\n"
    "v_dot2c_f32_bf16 v66, s77, v133\n"
    "v_dot2c_f32_bf16 v53, s77, v134\n"
    "v_dot2c_f32_bf16 v66, s77, v135\n"
    "v_dot2c_f32_bf16 v53, s77, v136\n"
    "v_dot2c_f32_bf16 v66, s77, v137\n"
    "v_dot2c_f32_bf16 v53, s77, v138\n"
    "v_dot2c_f32_bf16 v66, s77, v139\n"
    "v_dot2c_f32_bf16 v53, s77, v140\n"
    "v_dot2c_f32_bf16 v66, s77, v141\n"
    "v_dot2c_f32_bf16 v53, s77, v142\n"
    "v_dot2c_f32_bf16 v66, s77, v143\n"
    "v_dot2c_f32_bf16 v53, s77, v112\n"
    "v_dot2c_f32_bf16 v66, s77, v113\n"
    "v_dot2c_f32_bf16 v53, s77, v114\n"
    "v_dot2c_f32_bf16 v66, s77, v115\n"
    "v_dot2c_f32_bf16 v53, s77, v116\n"
    "v_dot2c_f32_bf16 v66, s77, v117\n"
    "v_dot2c_f32_bf16 v53, s77, v118\n"
    "v_dot2c_f32_bf16 v66, s77, v119\n"
    "v_dot2c_f32_bf16 v53, s77, v120\n"
    "v_dot2c_f32_bf16 v66, s77, v121\n"
    "v_dot2c_f32_bf16 v53, s77, v122\n"
    "v_dot2c_f32_bf16 v66, s77, v123\n"
    "v_dot2c_f32_bf16 v53, s77, v124\n"
    "v_dot2c_f32_bf16 v66, s77, v125\n"
    "v_dot2c_f32_bf16 v53, s77, v126\n"
    "v_dot2c_f32_bf16 v66, s77, v127\n"
    "s_waitcnt lgkmcnt(10)\n"
    "v_mfma_f32_32x32x16_bf16 v[192:207], v[240:243], a[72:75], v[192:207]\n"
    "ds_read_b64_tr_b16 v[232:233], v40 offset:32768\n"
    "ds_read_b64_tr_b16 v[234:235], v40 offset:33280\n"
    "v_add_f32 v53, v53, v66\n"
    "ds_write_b32 v45, v53 offset:5120\n"
    "v_cvt_pk_bf16_f32 v155, v171, v173\n"
    "ds_write_b128 v208, v[152:155]\n"
    "v_mfma_f32_32x32x16_bf16 v[192:207], v[244:247], a[88:91], v[192:207]\n"
    "ds_read_b64_tr_b16 v[236:237], v40 offset:40960\n"
    "ds_read_b64_tr_b16 v[238:239], v40 offset:41472\n"
    "v_cvt_pk_bf16_f32 v156, v175, v177\n"
    "v_cvt_pk_bf16_f32 v157, v179, v181\n"
    "v_cvt_pk_bf16_f32 v158, v183, v185\n"
    "v_cvt_pk_bf16_f32 v159, v187, v189\n"
    "ds_write_b128 v209, v[156:159]\n"
    "s_waitcnt lgkmcnt(9)\n"
    "v_mfma_f32_32x32x16_bf16 v[192:207], v[224:227], a[104:107], v[192:207]\n"
    "ds_read_b64_tr_b16 v[240:241], v40 offset:49152\n"
    "ds_read_b64_tr_b16 v[242:243], v40 offset:49664\n"
    "ds_read_b64_tr_b16 a[184:185], v210 offset:0\n"
    "ds_read_b64_tr_b16 a[186:187], v211 offset:256\n"
    "ds_read_b64_tr_b16 a[188:189], v210 offset:1024\n"
    "ds_read_b64_tr_b16 a[190:191], v211 offset:1280\n"
    "v_mfma_f32_32x32x16_bf16 v[192:207], v[228:231], a[120:123], v[192:207]\n"
    "ds_read_b64_tr_b16 v[244:245], v40 offset:57344\n"
    "ds_read_b64_tr_b16 v[246:247], v40 offset:57856\n"
    "buffer_store_dwordx4 a[176:179], v42, s[32:35], s89 offen offset:0 sc1\n"
    "s_waitcnt lgkmcnt(15)\n"
    "buffer_store_dwordx4 a[180:183], v42, s[32:35], s89 offen offset:1024 sc1\n"
    "s_waitcnt lgkmcnt(4)\n"
    "buffer_store_dwordx4 a[184:187], v42, s[32:35], s89 offen offset:2048 sc1\n"
    "s_waitcnt lgkmcnt(2)\n"
    "buffer_store_dwordx4 a[188:191], v42, s[32:35], s89 offen offset:3072 sc1\n"
    "s_lshl4_add_u32 s68, s60, s61\n"
    "s_cmp_eq_u32 s68, 15\n"
    "s_cselect_b32 s68, 0, 1072693248\n"
    "s_lshl_b32 s69, s62, 7\n"
    "v_add_u32 v1, s68, v49\n"
    "s_lshl_b32 s68, s63, 7\n"
    "s_sub_u32 s69, s69, s68\n"
    "s_add_u32 s69, s69, 28837888\n"
    "v_mov_b32 v70, s84\n"
    "buffer_store_dwordx2 v[70:71], v1, s[32:35], s69 offen sc1\n"
    "s_cmp_eq_u32 s63, 3\n"
    "s_cbranch_scc1 .Lshort\n"
    "s_mov_b32 s68, 4294885376\n"
    "v_xad_u32 v27, v27, 64, s68\n"
    "v_xad_u32 v38, v38, 64, s68\n"
    "v_xad_u32 v39, v39, 64, s68\n"
    "v_xad_u32 v40, v40, 64, s68\n"
    "s_barrier\n"
    "v_mfma_f32_32x32x16_bf16 v[192:207], v[232:235], a[76:79], v[192:207]\n"
    "ds_read_b64_tr_b16 v[128:129], v27 offset:0\n"
    "ds_read_b64_tr_b16 v[130:131], v27 offset:512\n"
    "s_add_u32 m0, s72, 81920\n"
    "ds_read_b128 v[248:251], v43 offset:5120\n"
    "s_waitcnt lgkmcnt(0)\n"
    "v_add_f32 v248, v248, v249\n"
    "v_add_f32 v250, v250, v251\n"
    "s_waitcnt vmcnt(9)\n"
    "s_add_u32 s58, s85, 1\n"
    "v_mov_b32 v54, s58\n"
    "buffer_store_dwordx2 v[54:55], v49, s[32:35], s31 offen sc1\n"
    "v_mfma_f32_32x32x16_bf16 v[192:207], v[236:239], a[92:95], v[192:207]\n"
    "ds_read_b64_tr_b16 v[132:133], v27 offset:8192\n"
    "ds_read_b64_tr_b16 v[134:135], v27 offset:8704\n"
    "buffer_load_dwordx4 v19, s[20:23], s49 offen offset:0 lds\n"
    "v_add_f32 v248, v248, v250\n"
    "v_and_b32 v249, 0xffff0000, v248\n"
    "v_sub_f32 v249, v248, v249\n"
    "v_mfma_f32_32x32x16_bf16 v[192:207], v[240:243], a[108:111], v[192:207]\n"
    "ds_read_b64_tr_b16 v[136:137], v27 offset:16384\n"
    "ds_read_b64_tr_b16 v[138:139], v27 offset:16896\n"
    "v_and_b32 v250, 0xffff0000, v249\n"
    "v_perm_b32 v60, v249, v248, s29\n"
    "v_sub_f32 v250, v249, v250\n"
    "v_perm_b32 v61, v12, v250, s30\n"
    "v_mfma_f32_32x32x16_bf16 v[192:207], v[244:247], a[124:127], v[192:207]\n"
    "ds_read_b64_tr_b16 v[140:141], v27 offset:24576\n"
    "ds_read_b64_tr_b16 v[142:143], v27 offset:25088\n"
    "buffer_load_dwordx4 v20, s[20:23], s49 offen offset:1024 lds\n"
    "v_mfma_f32_32x32x16_bf16 v[96:111], v[60:63], v[56:59], v[192:207]\n"
    "s_waitcnt lgkmcnt(4)\n"
    "v_mfma_f32_32x32x16_bf16 v[192:207], v[128:131], a[0:3], 0\n"
    "ds_read_b64_tr_b16 v[224:225], v38 offset:0\n"
    "ds_read_b64_tr_b16 v[226:227], v38 offset:512\n"
    "s_add_u32 s66, s78, 1\n"
    "s_lshl_b32 s66, s66, 11\n"
    "s_add_u32 s66, s66, s28\n"
    "v_mfma_f32_32x32x16_bf16 v[192:207], v[132:135], a[16:19], v[192:207]\n"
    "ds_read_b64_tr_b16 v[228:229], v38 offset:8192\n"
    "ds_read_b64_tr_b16 v[230:231], v38 offset:8704\n"
    "buffer_load_dwordx4 v21, s[20:23], s49 offen offset:2048 lds\n"
    "s_waitcnt lgkmcnt(4)\n"
    "v_mfma_f32_32x32x16_bf16 v[192:207], v[136:139], a[32:35], v[192:207]\n"
    "ds_read_b64_tr_b16 v[232:233], v38 offset:16384\n"
    "ds_read_b64_tr_b16 v[234:235], v38 offset:16896\n"
    "v_mfma_f32_32x32x16_bf16 v[192:207], v[140:143], a[48:51], v[192:207]\n"
    "ds_read_b64_tr_b16 v[236:237], v38 offset:24576\n"
    "ds_read_b64_tr_b16 v[238:239], v38 offset:25088\n"
    "buffer_load_dwordx4 v22, s[20:23], s49 offen offset:3072 lds\n"
    "s_xor_b32 s74, s64, 2\n"
    "s_lshl_b32 s74, s74, 15\n"
    "v_pk_mul_f32 v[96:97], v[96:97], v[64:65] op_sel_hi:[1,0]\n"
    "v_max_f32 v96, 0, v96\n"
    "v_max_f32 v97, 0, v97\n"
    "v_pk_mul_f32 v[96:97], v[96:97], v[96:97]\n"
    "v_cvt_pk_bf16_f32 v88, v96, v97\n"
    "v_pk_mul_f32 v[98:99], v[98:99], v[64:65] op_sel_hi:[1,0]\n"
    "v_max_f32 v98, 0, v98\n"
    "v_max_f32 v99, 0, v99\n"
    "v_pk_mul_f32 v[98:99], v[98:99], v[98:99]\n"
    "v_cvt_pk_bf16_f32 v89, v98, v99\n"
    "v_pk_mul_f32 v[100:101], v[100:101], v[64:65] op_sel_hi:[1,0]\n"
    "v_max_f32 v100, 0, v100\n"
    "v_max_f32 v101, 0, v101\n"
    "v_pk_mul_f32 v[100:101], v[100:101], v[100:101]\n"
    "v_cvt_pk_bf16_f32 v90, v100, v101\n"
    "v_pk_mul_f32 v[102:103], v[102:103], v[64:65] op_sel_hi:[1,0]\n"
    "v_max_f32 v102, 0, v102\n"
    "v_max_f32 v103, 0, v103\n"
    "v_pk_mul_f32 v[102:103], v[102:103], v[102:103]\n"
    "v_cvt_pk_bf16_f32 v91, v102, v103\n"
    "buffer_store_dwordx4 v[88:91], v42, s[32:35], s66 offen offset:0 sc1\n"
    "v_pk_mul_f32 v[104:105], v[104:105], v[64:65] op_sel_hi:[1,0]\n"
    "v_max_f32 v104, 0, v104\n"
    "v_max_f32 v105, 0, v105\n"
    "v_pk_mul_f32 v[104:105], v[104:105], v[104:105]\n"
    "v_cvt_pk_bf16_f32 v92, v104, v105\n"
    "v_pk_mul_f32 v[106:107], v[106:107], v[64:65] op_sel_hi:[1,0]\n"
    "v_max_f32 v106, 0, v106\n"
    "v_max_f32 v107, 0, v107\n"
    "v_pk_mul_f32 v[106:107], v[106:107], v[106:107]\n"
    "v_cvt_pk_bf16_f32 v93, v106, v107\n"
    "v_pk_mul_f32 v[108:109], v[108:109], v[64:65] op_sel_hi:[1,0]\n"
    "v_max_f32 v108, 0, v108\n"
    "v_max_f32 v109, 0, v109\n"
    "v_pk_mul_f32 v[108:109], v[108:109], v[108:109]\n"
    "v_cvt_pk_bf16_f32 v94, v108, v109\n"
    "v_pk_mul_f32 v[110:111], v[110:111], v[64:65] op_sel_hi:[1,0]\n"
    "v_max_f32 v110, 0, v110\n"
    "v_max_f32 v111, 0, v111\n"
    "v_pk_mul_f32 v[110:111], v[110:111], v[110:111]\n"
    "v_cvt_pk_bf16_f32 v95, v110, v111\n"
    "buffer_store_dwordx4 v[92:95], v42, s[32:35], s66 offen offset:1024 sc1\n"
    "s_waitcnt lgkmcnt(4)\n"
    "v_mfma_f32_32x32x16_bf16 v[192:207], v[224:227], a[4:7], v[192:207]\n"
    "ds_read_b64_tr_b16 v[240:241], v39 offset:0\n"
    "ds_read_b64_tr_b16 v[242:243], v39 offset:512\n"
    "s_add_u32 m0, s72, 98304\n"
    "s_add_u32 s74, s74, s80\n"
    "v_mfma_f32_32x32x16_bf16 v[192:207], v[228:231], a[20:23], v[192:207]\n"
    "ds_read_b64_tr_b16 v[244:245], v39 offset:8192\n"
    "ds_read_b64_tr_b16 v[246:247], v39 offset:8704\n"
    "buffer_load_dwordx4 v23, s[24:27], s74 offen offset:0 lds\n"
    "s_waitcnt lgkmcnt(4)\n"
    "v_mfma_f32_32x32x16_bf16 v[192:207], v[232:235], a[36:39], v[192:207]\n"
    "ds_read_b64_tr_b16 v[224:225], v39 offset:16384\n"
    "ds_read_b64_tr_b16 v[226:227], v39 offset:16896\n"
    "v_mfma_f32_32x32x16_bf16 v[192:207], v[236:239], a[52:55], v[192:207]\n"
    "ds_read_b64_tr_b16 v[228:229], v39 offset:24576\n"
    "ds_read_b64_tr_b16 v[230:231], v39 offset:25088\n"
    "buffer_load_dwordx4 v24, s[24:27], s74 offen offset:1024 lds\n"
    "s_waitcnt lgkmcnt(4)\n"
    "v_mfma_f32_32x32x16_bf16 v[192:207], v[240:243], a[8:11], v[192:207]\n"
    "ds_read_b64_tr_b16 v[232:233], v40 offset:0\n"
    "ds_read_b64_tr_b16 v[234:235], v40 offset:512\n"
    "v_mfma_f32_32x32x16_bf16 v[192:207], v[244:247], a[24:27], v[192:207]\n"
    "ds_read_b64_tr_b16 v[236:237], v40 offset:8192\n"
    "ds_read_b64_tr_b16 v[238:239], v40 offset:8704\n"
    "buffer_load_dwordx4 v25, s[24:27], s74 offen offset:2048 lds\n"
    "s_waitcnt lgkmcnt(4)\n"
    "v_mfma_f32_32x32x16_bf16 v[192:207], v[224:227], a[40:43], v[192:207]\n"
    "ds_read_b64_tr_b16 v[240:241], v40 offset:16384\n"
    "ds_read_b64_tr_b16 v[242:243], v40 offset:16896\n"
    "v_mfma_f32_32x32x16_bf16 v[192:207], v[228:231], a[56:59], v[192:207]\n"
    "ds_read_b64_tr_b16 v[244:245], v40 offset:24576\n"
    "ds_read_b64_tr_b16 v[246:247], v40 offset:25088\n"
    "buffer_load_dwordx4 v26, s[24:27], s74 offen offset:3072 lds\n"
    "s_barrier\n"
    "s_waitcnt lgkmcnt(4)\n"
    "v_mfma_f32_32x32x16_bf16 v[192:207], v[232:235], a[12:15], v[192:207]\n"
    "ds_read_b64_tr_b16 v[112:113], v27 offset:32768\n"
    "ds_read_b64_tr_b16 v[114:115], v27 offset:33280\n"
    "s_xor_b32 s74, s64, 3\n"
    "s_lshl_b32 s74, s74, 15\n"
    "s_add_u32 m0, s72, 114688\n"
    "v_mfma_f32_32x32x16_bf16 v[192:207], v[236:239], a[28:31], v[192:207]\n"
    "ds_read_b64_tr_b16 v[116:117], v27 offset:40960\n"
    "ds_read_b64_tr_b16 v[118:119], v27 offset:41472\n"
    "s_add_u32 s74, s74, s80\n"
    "buffer_load_dwordx4 v23, s[24:27], s74 offen offset:0 lds\n"
    "s_waitcnt lgkmcnt(4)\n"
    "v_mfma_f32_32x32x16_bf16 v[192:207], v[240:243], a[44:47], v[192:207]\n"
    "ds_read_b64_tr_b16 v[120:121], v27 offset:49152\n"
    "ds_read_b64_tr_b16 v[122:123], v27 offset:49664\n"
    "v_mfma_f32_32x32x16_bf16 v[192:207], v[244:247], a[60:63], v[192:207]\n"
    "ds_read_b64_tr_b16 v[124:125], v27 offset:57344\n"
    "ds_read_b64_tr_b16 v[126:127], v27 offset:57856\n"
    "buffer_load_dwordx4 v24, s[24:27], s74 offen offset:1024 lds\n"
    "s_waitcnt lgkmcnt(4)\n"
    "v_mfma_f32_32x32x16_bf16 v[192:207], v[112:115], a[64:67], v[192:207]\n"
    "ds_read_b64_tr_b16 v[224:225], v38 offset:32768\n"
    "ds_read_b64_tr_b16 v[226:227], v38 offset:33280\n"
    "v_mfma_f32_32x32x16_bf16 v[192:207], v[116:119], a[80:83], v[192:207]\n"
    "ds_read_b64_tr_b16 v[228:229], v38 offset:40960\n"
    "ds_read_b64_tr_b16 v[230:231], v38 offset:41472\n"
    "buffer_load_dwordx4 v25, s[24:27], s74 offen offset:2048 lds\n"
    "s_waitcnt vmcnt(15)\n"
    "s_add_u32 s58, s85, 2\n"
    "v_mov_b32 v54, s58\n"
    "buffer_store_dwordx2 v[54:55], v49, s[32:35], s31 offen sc1\n"
    "s_waitcnt lgkmcnt(4)\n"
    "v_mfma_f32_32x32x16_bf16 v[192:207], v[120:123], a[96:99], v[192:207]\n"
    "ds_read_b64_tr_b16 v[232:233], v38 offset:49152\n"
    "ds_read_b64_tr_b16 v[234:235], v38 offset:49664\n"
    "v_mfma_f32_32x32x16_bf16 v[192:207], v[124:127], a[112:115], v[192:207]\n"
    "ds_read_b64_tr_b16 v[236:237], v38 offset:57344\n"
    "ds_read_b64_tr_b16 v[238:239], v38 offset:57856\n"
    "buffer_load_dwordx4 v26, s[24:27], s74 offen offset:3072 lds\n"
    "s_add_u32 m0, s72, 131072\n"
    "buffer_load_dwordx2 v[74:75], v48, s[32:35], s31 offen sc1\n"
    "s_waitcnt lgkmcnt(4)\n"
    "v_mfma_f32_32x32x16_bf16 v[192:207], v[224:227], a[68:71], v[192:207]\n"
    "ds_read_b64_tr_b16 v[240:241], v39 offset:32768\n"
    "ds_read_b64_tr_b16 v[242:243], v39 offset:33280\n"
    "v_mfma_f32_32x32x16_bf16 v[192:207], v[228:231], a[84:87], v[192:207]\n"
    "ds_read_b64_tr_b16 v[244:245], v39 offset:40960\n"
    "ds_read_b64_tr_b16 v[246:247], v39 offset:41472\n"
    "buffer_load_dwordx4 v19, s[20:23], s50 offen offset:0 lds\n"
    "s_waitcnt lgkmcnt(4)\n"
    "v_mfma_f32_32x32x16_bf16 v[192:207], v[232:235], a[100:103], v[192:207]\n"
    "ds_read_b64_tr_b16 v[224:225], v39 offset:49152\n"
    "ds_read_b64_tr_b16 v[226:227], v39 offset:49664\n"
    "v_mfma_f32_32x32x16_bf16 v[192:207], v[236:239], a[116:119], v[192:207]\n"
    "ds_read_b64_tr_b16 v[228:229], v39 offset:57344\n"
    "ds_read_b64_tr_b16 v[230:231], v39 offset:57856\n"
    "buffer_load_dwordx4 v20, s[20:23], s50 offen offset:1024 lds\n"
    "v_mov_b32 v53, 0\n"
    "v_mov_b32 v66, 0\n"
    "v_dot2c_f32_bf16 v53, s77, v128\n"
    "v_dot2c_f32_bf16 v66, s77, v129\n"
    "v_dot2c_f32_bf16 v53, s77, v130\n"
    "v_dot2c_f32_bf16 v66, s77, v131\n"
    "v_dot2c_f32_bf16 v53, s77, v132\n"
    "v_dot2c_f32_bf16 v66, s77, v133\n"
    "v_dot2c_f32_bf16 v53, s77, v134\n"
    "v_dot2c_f32_bf16 v66, s77, v135\n"
    "v_dot2c_f32_bf16 v53, s77, v136\n"
    "v_dot2c_f32_bf16 v66, s77, v137\n"
    "v_dot2c_f32_bf16 v53, s77, v138\n"
    "v_dot2c_f32_bf16 v66, s77, v139\n"
    "v_dot2c_f32_bf16 v53, s77, v140\n"
    "v_dot2c_f32_bf16 v66, s77, v141\n"
    "v_dot2c_f32_bf16 v53, s77, v142\n"
    "v_dot2c_f32_bf16 v66, s77, v143\n"
    "v_dot2c_f32_bf16 v53, s77, v112\n"
    "v_dot2c_f32_bf16 v66, s77, v113\n"
    "v_dot2c_f32_bf16 v53, s77, v114\n"
    "v_dot2c_f32_bf16 v66, s77, v115\n"
    "v_dot2c_f32_bf16 v53, s77, v116\n"
    "v_dot2c_f32_bf16 v66, s77, v117\n"
    "v_dot2c_f32_bf16 v53, s77, v118\n"
    "v_dot2c_f32_bf16 v66, s77, v119\n"
    "v_dot2c_f32_bf16 v53, s77, v120\n"
    "v_dot2c_f32_bf16 v66, s77, v121\n"
    "v_dot2c_f32_bf16 v53, s77, v122\n"
    "v_dot2c_f32_bf16 v66, s77, v123\n"
    "v_dot2c_f32_bf16 v53, s77, v124\n"
    "v_dot2c_f32_bf16 v66, s77, v125\n"
    "v_dot2c_f32_bf16 v53, s77, v126\n"
    "v_dot2c_f32_bf16 v66, s77, v127\n"
    "s_waitcnt lgkmcnt(4)\n"
    "v_mfma_f32_32x32x16_bf16 v[192:207], v[240:243], a[72:75], v[192:207]\n"
    "ds_read_b64_tr_b16 v[232:233], v40 offset:32768\n"
    "ds_read_b64_tr_b16 v[234:235], v40 offset:33280\n"
    "v_add_f32 v53, v53, v66\n"
    "ds_write_b32 v45, v53 offset:4096\n"
    "v_mfma_f32_32x32x16_bf16 v[192:207], v[244:247], a[88:91], v[192:207]\n"
    "ds_read_b64_tr_b16 v[236:237], v40 offset:40960\n"
    "ds_read_b64_tr_b16 v[238:239], v40 offset:41472\n"
    "buffer_load_dwordx4 v21, s[20:23], s50 offen offset:2048 lds\n"
    "s_waitcnt lgkmcnt(5)\n"
    "v_mfma_f32_32x32x16_bf16 v[192:207], v[224:227], a[104:107], v[192:207]\n"
    "ds_read_b64_tr_b16 v[240:241], v40 offset:49152\n"
    "ds_read_b64_tr_b16 v[242:243], v40 offset:49664\n"
    "v_mfma_f32_32x32x16_bf16 v[192:207], v[228:231], a[120:123], v[192:207]\n"
    "ds_read_b64_tr_b16 v[244:245], v40 offset:57344\n"
    "ds_read_b64_tr_b16 v[246:247], v40 offset:57856\n"
    "buffer_load_dwordx4 v22, s[20:23], s50 offen offset:3072 lds\n"
    "v_xor_b32 v27, 64, v27\n"
    "v_xor_b32 v38, 64, v38\n"
    "v_xor_b32 v39, 64, v39\n"
    "v_xor_b32 v40, 64, v40\n"
    "s_waitcnt lgkmcnt(6)\n"
    "s_barrier\n"
    "s_waitcnt vmcnt(14)\n"
    "s_add_u32 s58, s85, 3\n"
    "v_mov_b32 v54, s58\n"
    "buffer_store_dwordx2 v[54:55], v49, s[32:35], s31 offen sc1\n"
    "s_waitcnt lgkmcnt(4)\n"
    "v_mfma_f32_32x32x16_bf16 v[192:207], v[232:235], a[76:79], v[192:207]\n"
    "ds_read_b64_tr_b16 v[128:129], v27 offset:0\n"
    "ds_read_b64_tr_b16 v[130:131], v27 offset:512\n"
    "ds_read_b128 v[248:251], v43 offset:4096\n"
    "s_waitcnt lgkmcnt(0)\n"
    "v_add_f32 v248, v248, v249\n"
    "v_add_f32 v250, v250, v251\n"
    "v_mfma_f32_32x32x16_bf16 v[192:207], v[236:239], a[92:95], v[192:207]\n"
    "ds_read_b64_tr_b16 v[132:133], v27 offset:8192\n"
    "ds_read_b64_tr_b16 v[134:135], v27 offset:8704\n"
    "v_add_f32 v248, v248, v250\n"
    "v_and_b32 v249, 0xffff0000, v248\n"
    "v_sub_f32 v249, v248, v249\n"
    "v_and_b32 v250, 0xffff0000, v249\n"
    "v_mfma_f32_32x32x16_bf16 v[192:207], v[240:243], a[108:111], v[192:207]\n"
    "ds_read_b64_tr_b16 v[136:137], v27 offset:16384\n"
    "ds_read_b64_tr_b16 v[138:139], v27 offset:16896\n"
    "v_perm_b32 v60, v249, v248, s29\n"
    "v_sub_f32 v250, v249, v250\n"
    "v_perm_b32 v61, v13, v250, s30\n"
    "v_mfma_f32_32x32x16_bf16 v[192:207], v[244:247], a[124:127], v[192:207]\n"
    "ds_read_b64_tr_b16 v[140:141], v27 offset:24576\n"
    "ds_read_b64_tr_b16 v[142:143], v27 offset:25088\n"
    "v_mfma_f32_32x32x16_bf16 v[96:111], v[60:63], v[56:59], v[192:207]\n"
    "s_waitcnt lgkmcnt(4)\n"
    "v_mfma_f32_32x32x16_bf16 v[192:207], v[128:131], a[0:3], 0\n"
    "ds_read_b64_tr_b16 v[224:225], v38 offset:0\n"
    "ds_read_b64_tr_b16 v[226:227], v38 offset:512\n"
    "v_mfma_f32_32x32x16_bf16 v[192:207], v[132:135], a[16:19], v[192:207]\n"
    "ds_read_b64_tr_b16 v[228:229], v38 offset:8192\n"
    "ds_read_b64_tr_b16 v[230:231], v38 offset:8704\n"
    "s_waitcnt lgkmcnt(4)\n"
    "v_mfma_f32_32x32x16_bf16 v[192:207], v[136:139], a[32:35], v[192:207]\n"
    "ds_read_b64_tr_b16 v[232:233], v38 offset:16384\n"
    "ds_read_b64_tr_b16 v[234:235], v38 offset:16896\n"
    "v_mfma_f32_32x32x16_bf16 v[192:207], v[140:143], a[48:51], v[192:207]\n"
    "ds_read_b64_tr_b16 v[236:237], v38 offset:24576\n"
    "ds_read_b64_tr_b16 v[238:239], v38 offset:25088\n"
    "s_waitcnt lgkmcnt(4)\n"
    "v_mfma_f32_32x32x16_bf16 v[192:207], v[224:227], a[4:7], v[192:207]\n"
    "ds_read_b64_tr_b16 v[240:241], v39 offset:0\n"
    "ds_read_b64_tr_b16 v[242:243], v39 offset:512\n"
    "v_mfma_f32_32x32x16_bf16 v[192:207], v[228:231], a[20:23], v[192:207]\n"
    "ds_read_b64_tr_b16 v[244:245], v39 offset:8192\n"
    "ds_read_b64_tr_b16 v[246:247], v39 offset:8704\n"
    "s_add_u32 s66, s78, 2\n"
    "s_lshl_b32 s66, s66, 11\n"
    "s_add_u32 s66, s66, s28\n"
    "s_waitcnt lgkmcnt(4)\n"
    "v_mfma_f32_32x32x16_bf16 v[192:207], v[232:235], a[36:39], v[192:207]\n"
    "ds_read_b64_tr_b16 v[224:225], v39 offset:16384\n"
    "ds_read_b64_tr_b16 v[226:227], v39 offset:16896\n"
    "v_mfma_f32_32x32x16_bf16 v[192:207], v[236:239], a[52:55], v[192:207]\n"
    "ds_read_b64_tr_b16 v[228:229], v39 offset:24576\n"
    "ds_read_b64_tr_b16 v[230:231], v39 offset:25088\n"
    "v_mul_f32 v96, v64, v96\n"
    "v_mul_f32 v97, v64, v97\n"
    "v_max_f32 v96, 0, v96\n"
    "v_max_f32 v97, 0, v97\n"
    "s_waitcnt lgkmcnt(4)\n"
    "v_mfma_f32_32x32x16_bf16 v[192:207], v[240:243], a[8:11], v[192:207]\n"
    "ds_read_b64_tr_b16 v[232:233], v40 offset:0\n"
    "ds_read_b64_tr_b16 v[234:235], v40 offset:512\n"
    "v_mul_f32 v96, v96, v96\n"
    "v_mul_f32 v97, v97, v97\n"
    "v_cvt_pk_bf16_f32 v208, v96, v97\n"
    "buffer_load_dwordx2 v[76:77], v48, s[32:35], s31 offen sc1\n"
    "v_mfma_f32_32x32x16_bf16 v[192:207], v[244:247], a[24:27], v[192:207]\n"
    "ds_read_b64_tr_b16 v[236:237], v40 offset:8192\n"
    "ds_read_b64_tr_b16 v[238:239], v40 offset:8704\n"
    "v_mul_f32 v98, v64, v98\n"
    "v_mul_f32 v99, v64, v99\n"
    "v_max_f32 v98, 0, v98\n"
    "v_max_f32 v99, 0, v99\n"
    ".Lck1:\n"
    "s_add_u32 s58, s85, 1\n"
    "s_waitcnt vmcnt(6)\n"
    "v_subrev_u32 v67, s58, v74\n"
    "v_cmp_eq_u32 s[56:57], s86, v75\n"
    "v_cmp_ge_u32 vcc, 6, v67\n"
    "s_nop 0\n"
    "s_and_b32 s58, vcc_lo, s56\n"
    "s_and_b32 s58, s58, 7\n"
    "s_cmp_eq_u32 s58, 7\n"
    "s_cbranch_scc0 .Lsl1\n"
    ".Lok1:\n"
    "s_waitcnt lgkmcnt(4)\n"
    "v_mfma_f32_32x32x16_bf16 v[192:207], v[224:227], a[40:43], v[192:207]\n"
    "ds_read_b64_tr_b16 v[240:241], v40 offset:16384\n"
    "ds_read_b64_tr_b16 v[242:243], v40 offset:16896\n"
    "v_mul_f32 v98, v98, v98\n"
    "v_mul_f32 v99, v99, v99\n"
    "v_cvt_pk_bf16_f32 v209, v98, v99\n"
    "v_mfma_f32_32x32x16_bf16 v[192:207], v[228:231], a[56:59], v[192:207]\n"
    "ds_read_b64_tr_b16 v[244:245], v40 offset:24576\n"
    "ds_read_b64_tr_b16 v[246:247], v40 offset:25088\n"
    "buffer_load_dwordx4 a[128:131], v42, s[32:35], s42 offen offset:0 sc1\n"
    "v_mul_f32 v100, v64, v100\n"
    "v_mul_f32 v101, v64, v101\n"
    "v_max_f32 v100, 0, v100\n"
    "s_waitcnt lgkmcnt(4)\n"
    "v_mfma_f32_32x32x16_bf16 v[192:207], v[232:235], a[12:15], v[192:207]\n"
    "ds_read_b64_tr_b16 v[112:113], v27 offset:32768\n"
    "ds_read_b64_tr_b16 v[114:115], v27 offset:33280\n"
    "v_max_f32 v101, 0, v101\n"
    "v_mul_f32 v100, v100, v100\n"
    "v_mul_f32 v101, v101, v101\n"
    "v_mfma_f32_32x32x16_bf16 v[192:207], v[236:239], a[28:31], v[192:207]\n"
    "ds_read_b64_tr_b16 v[116:117], v27 offset:40960\n"
    "ds_read_b64_tr_b16 v[118:119], v27 offset:41472\n"
    "buffer_load_dwordx4 a[132:135], v42, s[32:35], s42 offen offset:1024 sc1\n"
    "v_cvt_pk_bf16_f32 v210, v100, v101\n"
    "v_mul_f32 v102, v64, v102\n"
    "v_mul_f32 v103, v64, v103\n"
    "s_waitcnt lgkmcnt(4)\n"
    "v_mfma_f32_32x32x16_bf16 v[192:207], v[240:243], a[44:47], v[192:207]\n"
    "ds_read_b64_tr_b16 v[120:121], v27 offset:49152\n"
    "ds_read_b64_tr_b16 v[122:123], v27 offset:49664\n"
    "v_max_f32 v102, 0, v102\n"
    "v_max_f32 v103, 0, v103\n"
    "v_mul_f32 v102, v102, v102\n"
    "v_mfma_f32_32x32x16_bf16 v[192:207], v[244:247], a[60:63], v[192:207]\n"
    "ds_read_b64_tr_b16 v[124:125], v27 offset:57344\n"
    "ds_read_b64_tr_b16 v[126:127], v27 offset:57856\n"
    "buffer_load_dwordx4 a[144:147], v42, s[32:35], s43 offen offset:0 sc1\n"
    "v_mul_f32 v103, v103, v103\n"
    "v_cvt_pk_bf16_f32 v211, v102, v103\n"
    "buffer_store_dwordx4 v[208:211], v42, s[32:35], s66 offen offset:0 sc1\n"
    "s_waitcnt lgkmcnt(4)\n"
    "v_mfma_f32_32x32x16_bf16 v[192:207], v[112:115], a[64:67], v[192:207]\n"
    "ds_read_b64_tr_b16 v[224:225], v38 offset:32768\n"
    "ds_read_b64_tr_b16 v[226:227], v38 offset:33280\n"
    "v_mul_f32 v104, v64, v104\n"
    "v_mul_f32 v105, v64, v105\n"
    "v_max_f32 v104, 0, v104\n"
    "v_mfma_f32_32x32x16_bf16 v[192:207], v[116:119], a[80:83], v[192:207]\n"
    "ds_read_b64_tr_b16 v[228:229], v38 offset:40960\n"
    "ds_read_b64_tr_b16 v[230:231], v38 offset:41472\n"
    "buffer_load_dwordx4 a[148:151], v42, s[32:35], s43 offen offset:1024 sc1\n"
    "v_max_f32 v105, 0, v105\n"
    "v_mul_f32 v104, v104, v104\n"
    "v_mul_f32 v105, v105, v105\n"
    "s_waitcnt lgkmcnt(4)\n"
    "v_mfma_f32_32x32x16_bf16 v[192:207], v[120:123], a[96:99], v[192:207]\n"
    "ds_read_b64_tr_b16 v[232:233], v38 offset:49152\n"
    "ds_read_b64_tr_b16 v[234:235], v38 offset:49664\n"
    "v_cvt_pk_bf16_f32 v212, v104, v105\n"
    "v_mul_f32 v106, v64, v106\n"
    "v_mul_f32 v107, v64, v107\n"
    "v_mfma_f32_32x32x16_bf16 v[192:207], v[124:127], a[112:115], v[192:207]\n"
    "ds_read_b64_tr_b16 v[236:237], v38 offset:57344\n"
    "ds_read_b64_tr_b16 v[238:239], v38 offset:57856\n"
    "buffer_load_dwordx4 a[160:163], v42, s[32:35], s44 offen offset:0 sc1\n"
    "v_max_f32 v106, 0, v106\n"
    "v_max_f32 v107, 0, v107\n"
    "v_mul_f32 v106, v106, v106\n"
    "s_waitcnt lgkmcnt(4)\n"
    "v_mfma_f32_32x32x16_bf16 v[192:207], v[224:227], a[68:71], v[192:207]\n"
    "ds_read_b64_tr_b16 v[240:241], v39 offset:32768\n"
    "ds_read_b64_tr_b16 v[242:243], v39 offset:33280\n"
    "v_mul_f32 v107, v107, v107\n"
    "v_cvt_pk_bf16_f32 v213, v106, v107\n"
    "v_mul_f32 v108, v64, v108\n"
    "v_mfma_f32_32x32x16_bf16 v[192:207], v[228:231], a[84:87], v[192:207]\n"
    "ds_read_b64_tr_b16 v[244:245], v39 offset:40960\n"
    "ds_read_b64_tr_b16 v[246:247], v39 offset:41472\n"
    "buffer_load_dwordx4 a[164:167], v42, s[32:35], s44 offen offset:1024 sc1\n"
    "v_mul_f32 v109, v64, v109\n"
    "v_max_f32 v108, 0, v108\n"
    "v_max_f32 v109, 0, v109\n"
    "buffer_load_dwordx2 v[74:75], v48, s[32:35], s31 offen sc1\n"
    "s_waitcnt lgkmcnt(4)\n"
    "v_mfma_f32_32x32x16_bf16 v[192:207], v[232:235], a[100:103], v[192:207]\n"
    "ds_read_b64_tr_b16 v[224:225], v39 offset:49152\n"
    "ds_read_b64_tr_b16 v[226:227], v39 offset:49664\n"
    "v_mul_f32 v108, v108, v108\n"
    "v_mul_f32 v109, v109, v109\n"
    "v_cvt_pk_bf16_f32 v214, v108, v109\n"
    "v_mfma_f32_32x32x16_bf16 v[192:207], v[236:239], a[116:119], v[192:207]\n"
    "ds_read_b64_tr_b16 v[228:229], v39 offset:57344\n"
    "ds_read_b64_tr_b16 v[230:231], v39 offset:57856\n"
    "v_mul_f32 v110, v64, v110\n"
    "v_mul_f32 v111, v64, v111\n"
    "v_max_f32 v110, 0, v110\n"
    "v_max_f32 v111, 0, v111\n"
    "v_mov_b32 v53, 0\n"
    "v_mov_b32 v66, 0\n"
    "v_dot2c_f32_bf16 v53, s77, v128\n"
    "v_dot2c_f32_bf16 v66, s77, v129\n"
    "v_dot2c_f32_bf16 v53, s77, v130\n"
    "v_dot2c_f32_bf16 v66, s77, v131\n"
    "v_dot2c_f32_bf16 v53, s77, v132\n"
    "v_dot2c_f32_bf16 v66, s77, v133\n"
    "v_dot2c_f32_bf16 v53, s77, v134\n"
    "v_dot2c_f32_bf16 v66, s77, v135\n"
    "v_dot2c_f32_bf16 v53, s77, v136\n"
    "v_dot2c_f32_bf16 v66, s77, v137\n"
    "v_dot2c_f32_bf16 v53, s77, v138\n"
    "v_dot2c_f32_bf16 v66, s77, v139\n"
    "v_dot2c_f32_bf16 v53, s77, v140\n"
    "v_dot2c_f32_bf16 v66, s77, v141\n"
    "v_dot2c_f32_bf16 v53, s77, v142\n"
    "v_dot2c_f32_bf16 v66, s77, v143\n"
    "v_dot2c_f32_bf16 v53, s77, v112\n"
    "v_dot2c_f32_bf16 v66, s77, v113\n"
    "v_dot2c_f32_bf16 v53, s77, v114\n"
    "v_dot2c_f32_bf16 v66, s77, v115\n"
    "v_dot2c_f32_bf16 v53, s77, v116\n"
    "v_dot2c_f32_bf16 v66, s77, v117\n"
    "v_dot2c_f32_bf16 v53, s77, v118\n"
    "v_dot2c_f32_bf16 v66, s77, v119\n"
    "v_dot2c_f32_bf16 v53, s77, v120\n"
    "v_dot2c_f32_bf16 v66, s77, v121\n"
    "v_dot2c_f32_bf16 v53, s77, v122\n"
    "v_dot2c_f32_bf16 v66, s77, v123\n"
    "v_dot2c_f32_bf16 v53, s77, v124\n"
    "v_dot2c_f32_bf16 v66, s77, v125\n"
    "v_dot2c_f32_bf16 v53, s77, v126\n"
    "v_dot2c_f32_bf16 v66, s77, v127\n"
    "s_waitcnt lgkmcnt(4)\n"
    "v_mfma_f32_32x32x16_bf16 v[192:207], v[240:243], a[72:75], v[192:207]\n"
    "ds_read_b64_tr_b16 v[232:233], v40 offset:32768\n"
    "ds_read_b64_tr_b16 v[234:235], v40 offset:33280\n"
    "v_add_f32 v53, v53, v66\n"
    "ds_write_b32 v45, v53 offset:5120\n"
    "v_mul_f32 v110, v110, v110\n"
    "v_mfma_f32_32x32x16_bf16 v[192:207], v[244:247], a[88:91], v[192:207]\n"
    "ds_read_b64_tr_b16 v[236:237], v40 offset:40960\n"
    "ds_read_b64_tr_b16 v[238:239], v40 offset:41472\n"
    "v_mul_f32 v111, v111, v111\n"
    "v_cvt_pk_bf16_f32 v215, v110, v111\n"
    "buffer_store_dwordx4 v[212:215], v42, s[32:35], s66 offen offset:1024 sc1\n"
    "v_add3_u32 v78, v208, v209, s86\n"
    "s_waitcnt lgkmcnt(5)\n"
    "v_mfma_f32_32x32x16_bf16 v[192:207], v[224:227], a[104:107], v[192:207]\n"
    "ds_read_b64_tr_b16 v[240:241], v40 offset:49152\n"
    "ds_read_b64_tr_b16 v[242:243], v40 offset:49664\n"
    "v_add3_u32 v78, v210, v211, v78\n"
    "v_add3_u32 v78, v212, v213, v78\n"
    "v_add3_u32 v78, v214, v215, v78\n"
    ".Lck2:\n"
    "s_add_u32 s58, s85, 2\n"
    "s_waitcnt vmcnt(9)\n"
    "v_subrev_u32 v67, s58, v76\n"
    "v_cmp_eq_u32 s[56:57], s86, v77\n"
    "v_cmp_ge_u32 vcc, 5, v67\n"
    "s_nop 0\n"
    "s_and_b32 s58, vcc_lo, s56\n"
    "s_and_b32 s58, s58, 7\n"
    "s_cmp_eq_u32 s58, 7\n"
    "s_cbranch_scc0 .Lsl2\n"
    ".Lok2:\n"
    "v_mfma_f32_32x32x16_bf16 v[192:207], v[228:231], a[120:123], v[192:207]\n"
    "ds_read_b64_tr_b16 v[244:245], v40 offset:57344\n"
    "ds_read_b64_tr_b16 v[246:247], v40 offset:57856\n"
    "s_branch .Ljoin\n"
    ".Lshort:\n"
    "s_waitcnt vmcnt(9)\n"
    "s_add_u32 s58, s85, 1\n"
    "v_mov_b32 v54, s58\n"
    "buffer_store_dwordx2 v[54:55], v49, s[32:35], s31 offen sc1\n"
    "s_waitcnt lgkmcnt(0)\n"
    "s_barrier\n"
    "v_mfma_f32_32x32x16_bf16 v[192:207], v[232:235], a[76:79], v[192:207]\n"
    "v_mfma_f32_32x32x16_bf16 v[192:207], v[236:239], a[92:95], v[192:207]\n"
    "v_mfma_f32_32x32x16_bf16 v[192:207], v[240:243], a[108:111], v[192:207]\n"
    "v_mfma_f32_32x32x16_bf16 v[192:207], v[244:247], a[124:127], v[192:207]\n"
    "ds_read_b128 v[248:251], v43 offset:5120\n"
    "s_waitcnt lgkmcnt(0)\n"
    "v_add_f32 v248, v248, v249\n"
    "v_add_f32 v250, v250, v251\n"
    "v_add_f32 v248, v248, v250\n"
    "v_and_b32 v249, 0xffff0000, v248\n"
    "v_sub_f32 v249, v248, v249\n"
    "v_and_b32 v250, 0xffff0000, v249\n"
    "v_perm_b32 v60, v249, v248, s29\n"
    "v_sub_f32 v250, v249, v250\n"
    "v_perm_b32 v61, v12, v250, s30\n"
    "s_nop 1\n"
    "v_mfma_f32_32x32x16_bf16 v[96:111], v[60:63], v[56:59], v[192:207]\n"
    "s_add_u32 s66, s78, 1\n"
    "s_lshl_b32 s66, s66, 11\n"
    "s_add_u32 s66, s66, s28\n"
    "s_nop 8\n"
    "v_pk_mul_f32 v[96:97], v[96:97], v[64:65] op_sel_hi:[1,0]\n"
    "v_max_f32 v96, 0, v96\n"
    "v_max_f32 v97, 0, v97\n"
    "v_pk_mul_f32 v[96:97], v[96:97], v[96:97]\n"
    "v_cvt_pk_bf16_f32 v88, v96, v97\n"
    "v_pk_mul_f32 v[98:99], v[98:99], v[64:65] op_sel_hi:[1,0]\n"
    "v_max_f32 v98, 0, v98\n"
    "v_max_f32 v99, 0, v99\n"
    "v_pk_mul_f32 v[98:99], v[98:99], v[98:99]\n"
    "v_cvt_pk_bf16_f32 v89, v98, v99\n"
    "v_pk_mul_f32 v[100:101], v[100:101], v[64:65] op_sel_hi:[1,0]\n"
    "v_max_f32 v100, 0, v100\n"
    "v_max_f32 v101, 0, v101\n"
    "v_pk_mul_f32 v[100:101], v[100:101], v[100:101]\n"
    "v_cvt_pk_bf16_f32 v90, v100, v101\n"
    "v_pk_mul_f32 v[102:103], v[102:103], v[64:65] op_sel_hi:[1,0]\n"
    "v_max_f32 v102, 0, v102\n"
    "v_max_f32 v103, 0, v103\n"
    "v_pk_mul_f32 v[102:103], v[102:103], v[102:103]\n"
    "v_cvt_pk_bf16_f32 v91, v102, v103\n"
    "buffer_store_dwordx4 v[88:91], v42, s[32:35], s66 offen offset:0 sc1\n"
    "v_pk_mul_f32 v[104:105], v[104:105], v[64:65] op_sel_hi:[1,0]\n"
    "v_max_f32 v104, 0, v104\n"
    "v_max_f32 v105, 0, v105\n"
    "v_pk_mul_f32 v[104:105], v[104:105], v[104:105]\n"
    "v_cvt_pk_bf16_f32 v92, v104, v105\n"
    "v_pk_mul_f32 v[106:107], v[106:107], v[64:65] op_sel_hi:[1,0]\n"
    "v_max_f32 v106, 0, v106\n"
    "v_max_f32 v107, 0, v107\n"
    "v_pk_mul_f32 v[106:107], v[106:107], v[106:107]\n"
    "v_cvt_pk_bf16_f32 v93, v106, v107\n"
    "v_pk_mul_f32 v[108:109], v[108:109], v[64:65] op_sel_hi:[1,0]\n"
    "v_max_f32 v108, 0, v108\n"
    "v_max_f32 v109, 0, v109\n"
    "v_pk_mul_f32 v[108:109], v[108:109], v[108:109]\n"
    "v_cvt_pk_bf16_f32 v94, v108, v109\n"
    "v_pk_mul_f32 v[110:111], v[110:111], v[64:65] op_sel_hi:[1,0]\n"
    "v_max_f32 v110, 0, v110\n"
    "v_max_f32 v111, 0, v111\n"
    "v_pk_mul_f32 v[110:111], v[110:111], v[110:111]\n"
    "v_cvt_pk_bf16_f32 v95, v110, v111\n"
    "buffer_store_dwordx4 v[92:95], v42, s[32:35], s66 offen offset:1024 sc1\n"
    "s_waitcnt vmcnt(0)\n"
    "s_add_u32 s58, s85, 3\n"
    "v_mov_b32 v54, s58\n"
    "buffer_store_dwordx2 v[54:55], v49, s[32:35], s31 offen sc1\n"
    "s_add_u32 m0, s72, 81920\n"
    "s_nop 0\n"
    "buffer_load_dwordx4 v19, s[20:23], s49 offen offset:0 lds\n"
    "buffer_load_dwordx4 v20, s[20:23], s49 offen offset:1024 lds\n"
    "buffer_load_dwordx4 v21, s[20:23], s49 offen offset:2048 lds\n"
    "buffer_load_dwordx4 v22, s[20:23], s49 offen offset:3072 lds\n"
    "s_xor_b32 s74, s64, 2\n"
    "s_lshl_b32 s74, s74, 15\n"
    "s_add_u32 m0, s72, 98304\n"
    "s_add_u32 s74, s74, s80\n"
    "buffer_load_dwordx4 v23, s[24:27], s74 offen offset:0 lds\n"
    "buffer_load_dwordx4 v24, s[24:27], s74 offen offset:1024 lds\n"
    "buffer_load_dwordx4 v25, s[24:27], s74 offen offset:2048 lds\n"
    "buffer_load_dwordx4 v26, s[24:27], s74 offen offset:3072 lds\n"
    "s_xor_b32 s74, s64, 3\n"
    "s_lshl_b32 s74, s74, 15\n"
    "s_add_u32 m0, s72, 114688\n"
    "s_add_u32 s74, s74, s80\n"
    "buffer_load_dwordx4 v23, s[24:27], s74 offen offset:0 lds\n"
    "buffer_load_dwordx4 v24, s[24:27], s74 offen offset:1024 lds\n"
    "buffer_load_dwordx4 v25, s[24:27], s74 offen offset:2048 lds\n"
    "buffer_load_dwordx4 v26, s[24:27], s74 offen offset:3072 lds\n"
    "s_add_u32 m0, s72, 131072\n"
    "s_nop 0\n"
    "buffer_load_dwordx4 v19, s[20:23], s50 offen offset:0 lds\n"
    "buffer_load_dwordx4 v20, s[20:23], s50 offen offset:1024 lds\n"
    "buffer_load_dwordx4 v21, s[20:23], s50 offen offset:2048 lds\n"
    "buffer_load_dwordx4 v22, s[20:23], s50 offen offset:3072 lds\n"
    "buffer_load_dwordx2 v[74:75], v48, s[32:35], s31 offen sc1\n"
    ".Lck3:\n"
    "s_add_u32 s58, s85, 2\n"
    "s_waitcnt vmcnt(0)\n"
    "v_subrev_u32 v67, s58, v74\n"
    "v_cmp_eq_u32 s[56:57], s86, v75\n"
    "v_cmp_ge_u32 vcc, 5, v67\n"
    "s_nop 0\n"
    "s_and_b32 s58, vcc_lo, s56\n"
    "s_and_b32 s58, s58, 7\n"
    "s_cmp_eq_u32 s58, 7\n"
    "s_cbranch_scc0 .Lsl3\n"
    ".Lok3:\n"
    "buffer_load_dwordx4 a[128:131], v42, s[32:35], s42 offen offset:0 sc1\n"
    "buffer_load_dwordx4 a[132:135], v42, s[32:35], s42 offen offset:1024 sc1\n"
    "buffer_load_dwordx4 a[144:147], v42, s[32:35], s43 offen offset:0 sc1\n"
    "buffer_load_dwordx4 a[148:151], v42, s[32:35], s43 offen offset:1024 sc1\n"
    "buffer_load_dwordx4 a[160:163], v42, s[32:35], s44 offen offset:0 sc1\n"
    "buffer_load_dwordx4 a[164:167], v42, s[32:35], s44 offen offset:1024 sc1\n"
    "buffer_load_dwordx2 v[74:75], v48, s[32:35], s31 offen sc1\n"
    ".Ljoin:\n"
    "s_and_b32 s68, s64, 1\n"
    "s_lshl_b32 s68, s68, 6\n"
    "s_add_u32 s68, s68, 65536\n"
    "v_lshl_add_u32 v1, v8, 9, v41\n"
    "v_xor_b32 v17, s68, v1\n"
    "v_xor_b32 v18, 64, v17\n"
    "v_bfe_u32 v1, v5, 1, 3\n"
    "v_xor_b32 v1, v1, v8\n"
    "v_lshlrev_b32 v2, 7, v7\n"
    "s_add_u32 s68, s63, 0\n"
    "s_and_b32 s69, s68, 1\n"
    "s_lshl_b32 s69, s69, 2\n"
    "s_and_b32 s68, s68, 2\n"
    "s_lshl_b32 s68, s68, 11\n"
    "s_add_u32 s68, s68, 65536\n"
    "v_xor_b32 v3, s69, v1\n"
    "v_lshl_add_u32 v3, v3, 4, v2\n"
    "v_add_u32 v29, s68, v3\n"
    "v_xor_b32 v30, 32, v29\n"
    "s_waitcnt lgkmcnt(6)\n"
    "s_barrier\n"
    "v_mfma_f32_32x32x16_bf16 v[192:207], v[232:235], a[76:79], v[192:207]\n"
    "ds_read_b64_tr_b16 v[224:225], v17 offset:0\n"
    "ds_read_b64_tr_b16 v[226:227], v17 offset:1024\n"
    "buffer_load_dwordx4 a[208:211], v42, s[32:35], s91 offen offset:0 sc1\n"
    "ds_read_b128 v[248:251], v43 offset:5120\n"
    "s_waitcnt lgkmcnt(7)\n"
    "v_mfma_f32_32x32x16_bf16 v[192:207], v[236:239], a[92:95], v[192:207]\n"
    "ds_read_b64_tr_b16 v[228:229], v18 offset:0\n"
    "ds_read_b64_tr_b16 v[230:231], v18 offset:1024\n"
    "buffer_load_dwordx4 a[212:215], v42, s[32:35], s91 offen offset:1024 sc1\n"
    "s_waitcnt lgkmcnt(2)\n"
    "v_add_f32 v248, v248, v249\n"
    "v_mfma_f32_32x32x16_bf16 v[192:207], v[240:243], a[108:111], v[192:207]\n"
    "ds_read_b64_tr_b16 v[232:233], v17 offset:2048\n"
    "ds_read_b64_tr_b16 v[234:235], v17 offset:3072\n"
    "buffer_load_dwordx4 a[216:219], v42, s[32:35], s91 offen offset:2048 sc1\n"
    "v_add_f32 v250, v250, v251\n"
    "v_mfma_f32_32x32x16_bf16 v[192:207], v[244:247], a[124:127], v[192:207]\n"
    "ds_read_b64_tr_b16 v[236:237], v18 offset:2048\n"
    "ds_read_b64_tr_b16 v[238:239], v18 offset:3072\n"
    "buffer_load_dwordx4 a[220:223], v42, s[32:35], s91 offen offset:3072 sc1\n"
    "v_add_f32 v248, v248, v250\n"
    "v_and_b32 v249, 0xffff0000, v248\n"
    "v_sub_f32 v249, v248, v249\n"
    "v_and_b32 v250, 0xffff0000, v249\n"
    "v_perm_b32 v60, v249, v248, s29\n"
    "v_sub_f32 v250, v249, v250\n"
    "v_perm_b32 v61, v14, v250, s30\n"
    "s_nop 1\n"
    "v_mfma_f32_32x32x16_bf16 v[96:111], v[60:63], v[56:59], v[192:207]\n"
    "s_waitcnt lgkmcnt(4)\n"
    "v_mfma_f32_32x32x16_bf16 v[160:175], v[80:83], v[224:227], 0\n"
    "ds_read_b64_tr_b16 v[240:241], v17 offset:4096\n"
    "ds_read_b64_tr_b16 v[242:243], v17 offset:5120\n"
    "buffer_load_dwordx4 a[224:227], v42, s[32:35], s92 offen offset:0 sc1\n"
    "v_mfma_f32_32x32x16_bf16 v[176:191], v[80:83], v[228:231], 0\n"
    "ds_read_b64_tr_b16 v[244:245], v18 offset:4096\n"
    "ds_read_b64_tr_b16 v[246:247], v18 offset:5120\n"
    "buffer_load_dwordx4 a[228:231], v42, s[32:35], s92 offen offset:1024 sc1\n"
    "s_add_u32 s66, s78, 3\n"
    "s_waitcnt lgkmcnt(4)\n"
    "v_mfma_f32_32x32x16_bf16 v[160:175], v[84:87], v[232:235], v[160:175]\n"
    "ds_read_b64_tr_b16 v[224:225], v17 offset:6144\n"
    "ds_read_b64_tr_b16 v[226:227], v17 offset:7168\n"
    "buffer_load_dwordx4 a[232:235], v42, s[32:35], s92 offen offset:2048 sc1\n"
    "v_mfma_f32_32x32x16_bf16 v[176:191], v[84:87], v[236:239], v[176:191]\n"
    "ds_read_b64_tr_b16 v[228:229], v18 offset:6144\n"
    "ds_read_b64_tr_b16 v[230:231], v18 offset:7168\n"
    "buffer_load_dwordx4 a[236:239], v42, s[32:35], s92 offen offset:3072 sc1\n"
    "s_lshl_b32 s66, s66, 11\n"
    "s_waitcnt lgkmcnt(4)\n"
    "v_mfma_f32_32x32x16_bf16 v[160:175], v[88:91], v[240:243], v[160:175]\n"
    "ds_read_b128 v[232:235], v29 offset:32768\n"
    "buffer_load_dwordx4 a[240:243], v42, s[32:35], s93 offen offset:0 sc1\n"
    "s_add_u32 s66, s66, s28\n"
    "v_mfma_f32_32x32x16_bf16 v[176:191], v[88:91], v[244:247], v[176:191]\n"
    "ds_read_b128 v[236:239], v29 offset:49152\n"
    "buffer_load_dwordx4 a[244:247], v42, s[32:35], s93 offen offset:1024 sc1\n"
    "s_cmp_eq_u32 s63, 3\n"
    "s_cselect_b32 s66, 1072693248, s66\n"
    "s_cselect_b32 s97, 1072693248, s97\n"
    "s_waitcnt lgkmcnt(2)\n"
    "v_mfma_f32_32x32x16_bf16 v[160:175], v[92:95], v[224:227], v[160:175]\n"
    "ds_read_b128 v[240:243], v30 offset:32768\n"
    "buffer_load_dwordx4 a[248:251], v42, s[32:35], s93 offen offset:2048 sc1\n"
    "v_mfma_f32_32x32x16_bf16 v[176:191], v[92:95], v[228:231], v[176:191]\n"
    "ds_read_b128 v[244:247], v30 offset:49152\n"
    "buffer_load_dwordx4 a[252:255], v42, s[32:35], s93 offen offset:3072 sc1\n"
    "s_waitcnt lgkmcnt(2)\n"
    "v_mfma_f32_32x32x16_bf16 v[160:175], a[176:179], v[232:235], v[160:175]\n"
    "ds_read_b128 v[224:227], v29 offset:40960\n"
    "v_pk_mul_f32 v[96:97], v[96:97], v[64:65] op_sel_hi:[1,0]\n"
    "v_max_f32 v96, 0, v96\n"
    "v_max_f32 v97, 0, v97\n"
    "v_pk_mul_f32 v[96:97], v[96:97], v[96:97]\n"
    "v_cvt_pk_bf16_f32 v216, v96, v97\n"
    "v_pk_mul_f32 v[98:99], v[98:99], v[64:65] op_sel_hi:[1,0]\n"
    "v_max_f32 v98, 0, v98\n"
    "v_max_f32 v99, 0, v99\n"
    "v_pk_mul_f32 v[98:99], v[98:99], v[98:99]\n"
    "v_cvt_pk_bf16_f32 v217, v98, v99\n"
    "v_pk_mul_f32 v[100:101], v[100:101], v[64:65] op_sel_hi:[1,0]\n"
    "v_max_f32 v100, 0, v100\n"
    "v_max_f32 v101, 0, v101\n"
    "v_pk_mul_f32 v[100:101], v[100:101], v[100:101]\n"
    "v_cvt_pk_bf16_f32 v218, v100, v101\n"
    "v_pk_mul_f32 v[102:103], v[102:103], v[64:65] op_sel_hi:[1,0]\n"
    "v_max_f32 v102, 0, v102\n"
    "v_max_f32 v103, 0, v103\n"
    "v_pk_mul_f32 v[102:103], v[102:103], v[102:103]\n"
    "v_cvt_pk_bf16_f32 v219, v102, v103\n"
    "buffer_store_dwordx4 v[216:219], v42, s[32:35], s66 offen offset:0 sc1\n"
    "v_pk_mul_f32 v[104:105], v[104:105], v[64:65] op_sel_hi:[1,0]\n"
    "v_max_f32 v104, 0, v104\n"
    "v_max_f32 v105, 0, v105\n"
    "v_pk_mul_f32 v[104:105], v[104:105], v[104:105]\n"
    "v_cvt_pk_bf16_f32 v220, v104, v105\n"
    "v_pk_mul_f32 v[106:107], v[106:107], v[64:65] op_sel_hi:[1,0]\n"
    "v_max_f32 v106, 0, v106\n"
    "v_max_f32 v107, 0, v107\n"
    "v_pk_mul_f32 v[106:107], v[106:107], v[106:107]\n"
    "v_cvt_pk_bf16_f32 v221, v106, v107\n"
    "v_pk_mul_f32 v[108:109], v[108:109], v[64:65] op_sel_hi:[1,0]\n"
    "v_max_f32 v108, 0, v108\n"
    "v_max_f32 v109, 0, v109\n"
    "v_pk_mul_f32 v[108:109], v[108:109], v[108:109]\n"
    "v_cvt_pk_bf16_f32 v222, v108, v109\n"
    "v_pk_mul_f32 v[110:111], v[110:111], v[64:65] op_sel_hi:[1,0]\n"
    "v_max_f32 v110, 0, v110\n"
    "v_max_f32 v111, 0, v111\n"
    "v_pk_mul_f32 v[110:111], v[110:111], v[110:111]\n"
    "v_cvt_pk_bf16_f32 v223, v110, v111\n"
    "buffer_store_dwordx4 v[220:223], v42, s[32:35], s66 offen offset:1024 sc1\n"
    "v_add3_u32 v78, v216, v217, v78\n"
    "v_add3_u32 v78, v218, v219, v78\n"
    "v_add3_u32 v78, v220, v221, v78\n"
    "v_add3_u32 v78, v222, v223, v78\n"
    "buffer_store_dword v78, v52, s[32:35], s97 offen sc1\n"
    "v_mfma_f32_32x32x16_bf16 v[176:191], a[176:179], v[236:239], v[176:191]\n"
    "ds_read_b128 v[228:231], v29 offset:57344\n"
    "s_add_u32 s68, s63, 1\n"
    "s_and_b32 s69, s68, 1\n"
    "s_lshl_b32 s69, s69, 2\n"
    "s_waitcnt lgkmcnt(2)\n"
    "v_mfma_f32_32x32x16_bf16 v[160:175], a[180:183], v[240:243], v[160:175]\n"
    "ds_read_b128 v[232:235], v30 offset:40960\n"
    "s_and_b32 s68, s68, 2\n"
    "s_lshl_b32 s68, s68, 11\n"
    "v_mfma_f32_32x32x16_bf16 v[176:191], a[180:183], v[244:247], v[176:191]\n"
    "ds_read_b128 v[236:239], v30 offset:57344\n"
    "s_add_u32 s68, s68, 65536\n"
    "v_xor_b32 v3, s69, v1\n"
    "v_lshl_add_u32 v3, v3, 4, v2\n"
    "s_waitcnt lgkmcnt(2)\n"
    "v_mfma_f32_32x32x16_bf16 v[160:175], a[184:187], v[224:227], v[160:175]\n"
    "ds_read_b64_tr_b16 v[240:241], v17 offset:8192\n"
    "ds_read_b64_tr_b16 v[242:243], v17 offset:9216\n"
    "v_add_u32 v31, s68, v3\n"
    "v_mfma_f32_32x32x16_bf16 v[176:191], a[184:187], v[228:231], v[176:191]\n"
    "ds_read_b64_tr_b16 v[244:245], v18 offset:8192\n"
    "ds_read_b64_tr_b16 v[246:247], v18 offset:9216\n"
    "v_xor_b32 v32, 32, v31\n"
    "s_add_u32 s68, s63, 2\n"
    "s_waitcnt lgkmcnt(4)\n"
    "v_mfma_f32_32x32x16_bf16 v[160:175], a[188:191], v[232:235], v[160:175]\n"
    "ds_read_b64_tr_b16 v[224:225], v17 offset:10240\n"
    "ds_read_b64_tr_b16 v[226:227], v17 offset:11264\n"
    "s_and_b32 s69, s68, 1\n"
    "v_mfma_f32_32x32x16_bf16 v[176:191], a[188:191], v[236:239], v[176:191]\n"
    "ds_read_b64_tr_b16 v[228:229], v18 offset:10240\n"
    "ds_read_b64_tr_b16 v[230:231], v18 offset:11264\n"
    "s_lshl_b32 s69, s69, 2\n"
    "s_and_b32 s68, s68, 2\n"
    "s_waitcnt vmcnt(21) lgkmcnt(4)\n"
    "v_mfma_f32_32x32x16_bf16 v[160:175], a[128:131], v[240:243], v[160:175]\n"
    "ds_read_b64_tr_b16 v[232:233], v17 offset:16384\n"
    "ds_read_b64_tr_b16 v[234:235], v17 offset:17408\n"
    "s_lshl_b32 s68, s68, 11\n"
    "v_mfma_f32_32x32x16_bf16 v[176:191], a[128:131], v[244:247], v[176:191]\n"
    "ds_read_b64_tr_b16 v[236:237], v18 offset:16384\n"
    "ds_read_b64_tr_b16 v[238:239], v18 offset:17408\n"
    "s_add_u32 s68, s68, 65536\n"
    "v_xor_b32 v3, s69, v1\n"
    "s_waitcnt vmcnt(20) lgkmcnt(4)\n"
    "v_mfma_f32_32x32x16_bf16 v[160:175], a[132:135], v[224:227], v[160:175]\n"
    "ds_read_b64_tr_b16 v[240:241], v17 offset:18432\n"
    "ds_read_b64_tr_b16 v[242:243], v17 offset:19456\n"
    "v_lshl_add_u32 v3, v3, 4, v2\n"
    "v_mfma_f32_32x32x16_bf16 v[176:191], a[132:135], v[228:231], v[176:191]\n"
    "ds_read_b64_tr_b16 v[244:245], v18 offset:18432\n"
    "ds_read_b64_tr_b16 v[246:247], v18 offset:19456\n"
    "v_add_u32 v33, s68, v3\n"
    "v_xor_b32 v34, 32, v33\n"
    "s_waitcnt vmcnt(19) lgkmcnt(4)\n"
    "v_mfma_f32_32x32x16_bf16 v[160:175], a[144:147], v[232:235], v[160:175]\n"
    "ds_read_b64_tr_b16 v[224:225], v17 offset:24576\n"
    "ds_read_b64_tr_b16 v[226:227], v17 offset:25600\n"
    "s_add_u32 s68, s63, 3\n"
    "v_mfma_f32_32x32x16_bf16 v[176:191], a[144:147], v[236:239], v[176:191]\n"
    "ds_read_b64_tr_b16 v[228:229], v18 offset:24576\n"
    "ds_read_b64_tr_b16 v[230:231], v18 offset:25600\n"
    "s_and_b32 s69, s68, 1\n"
    "s_lshl_b32 s69, s69, 2\n"
    "s_barrier\n"
    ".Lck4:\n"
    "s_add_u32 s58, s85, 3\n"
    "s_waitcnt vmcnt(15)\n"
    "v_subrev_u32 v67, s58, v74\n"
    "v_cmp_eq_u32 s[56:57], s86, v75\n"
    "v_cmp_ge_u32 vcc, 4, v67\n"
    "s_nop 0\n"
    "s_and_b32 s58, vcc_lo, s56\n"
    "s_and_b32 s58, s58, 7\n"
    "s_cmp_eq_u32 s58, 7\n"
    "s_cbranch_scc0 .Lsl4\n"
    ".Lok4:\n"
    "s_waitcnt lgkmcnt(4)\n"
    "v_mfma_f32_32x32x16_bf16 v[160:175], a[148:151], v[240:243], v[160:175]\n"
    "ds_read_b64_tr_b16 v[232:233], v17 offset:26624\n"
    "ds_read_b64_tr_b16 v[234:235], v17 offset:27648\n"
    "s_and_b32 s68, s68, 2\n"
    "v_mfma_f32_32x32x16_bf16 v[176:191], a[148:151], v[244:247], v[176:191]\n"
    "ds_read_b64_tr_b16 v[236:237], v18 offset:26624\n"
    "ds_read_b64_tr_b16 v[238:239], v18 offset:27648\n"
    "buffer_load_dwordx4 a[136:139], v42, s[32:35], s42 offen offset:2048 sc1\n"
    "s_lshl_b32 s68, s68, 11\n"
    "s_add_u32 s68, s68, 65536\n"
    "v_xor_b32 v3, s69, v1\n"
    "v_lshl_add_u32 v3, v3, 4, v2\n"
    "v_add_u32 v35, s68, v3\n"
    "v_xor_b32 v36, 32, v35\n"
    "v_add_u32 v27, 65536, v17\n"
    "v_subrev_u32 v15, 65536, v17\n"
    "v_add_u32 v28, 65536, v18\n"
    "v_subrev_u32 v16, 65536, v18\n"
    "s_lshl2_add_u32 s13, s61, s63\n"
    "s_lshl_b32 s13, s13, 18\n"
    "s_add_u32 s13, s13, s72\n"
    "s_add_u32 s13, s13, 11534336\n"
    "s_waitcnt lgkmcnt(4)\n"
    "v_mfma_f32_32x32x16_bf16 v[160:175], a[160:163], v[224:227], v[160:175]\n"
    "ds_read_b128 v[240:243], v31 offset:32768\n"
    "v_mfma_f32_32x32x16_bf16 v[176:191], a[160:163], v[228:231], v[176:191]\n"
    "ds_read_b128 v[244:247], v31 offset:49152\n"
    "buffer_load_dwordx4 a[140:143], v42, s[32:35], s42 offen offset:3072 sc1\n"
    "s_waitcnt lgkmcnt(2)\n"
    "v_mfma_f32_32x32x16_bf16 v[160:175], a[164:167], v[232:235], v[160:175]\n"
    "ds_read_b128 v[224:227], v32 offset:32768\n"
    "v_mfma_f32_32x32x16_bf16 v[176:191], a[164:167], v[236:239], v[176:191]\n"
    "ds_read_b128 v[228:231], v32 offset:49152\n"
    "buffer_load_dwordx4 a[152:155], v42, s[32:35], s43 offen offset:2048 sc1\n"
    "s_waitcnt vmcnt(17) lgkmcnt(2)\n"
    "v_mfma_f32_32x32x16_bf16 v[160:175], a[208:211], v[240:243], v[160:175]\n"
    "ds_read_b128 v[232:235], v31 offset:40960\n"
    "v_mfma_f32_32x32x16_bf16 v[176:191], a[208:211], v[244:247], v[176:191]\n"
    "ds_read_b128 v[236:239], v31 offset:57344\n"
    "buffer_load_dwordx4 a[156:159], v42, s[32:35], s43 offen offset:3072 sc1\n"
    "s_waitcnt vmcnt(17) lgkmcnt(2)\n"
    "v_mfma_f32_32x32x16_bf16 v[160:175], a[212:215], v[224:227], v[160:175]\n"
    "ds_read_b128 v[240:243], v32 offset:40960\n"
    "v_mfma_f32_32x32x16_bf16 v[176:191], a[212:215], v[228:231], v[176:191]\n"
    "ds_read_b128 v[244:247], v32 offset:57344\n"
    "buffer_load_dwordx4 a[168:171], v42, s[32:35], s44 offen offset:2048 sc1\n"
    "s_waitcnt vmcnt(17) lgkmcnt(2)\n"
    "v_mfma_f32_32x32x16_bf16 v[160:175], a[216:219], v[232:235], v[160:175]\n"
    "ds_read_b128 v[224:227], v33 offset:32768\n"
    "v_mfma_f32_32x32x16_bf16 v[176:191], a[216:219], v[236:239], v[176:191]\n"
    "ds_read_b128 v[228:231], v33 offset:49152\n"
    "buffer_load_dwordx4 a[172:175], v42, s[32:35], s44 offen offset:3072 sc1\n"
    "s_add_u32 m0, s72, 32768\n"
    "s_waitcnt vmcnt(17) lgkmcnt(2)\n"
    "v_mfma_f32_32x32x16_bf16 v[160:175], a[220:223], v[240:243], v[160:175]\n"
    "ds_read_b128 v[232:235], v34 offset:32768\n"
    "v_mfma_f32_32x32x16_bf16 v[176:191], a[220:223], v[244:247], v[176:191]\n"
    "ds_read_b128 v[236:239], v34 offset:49152\n"
    "buffer_load_dwordx4 v19, s[20:23], s53 offen offset:0 lds\n"
    "s_waitcnt vmcnt(17) lgkmcnt(2)\n"
    "v_mfma_f32_32x32x16_bf16 v[160:175], a[224:227], v[224:227], v[160:175]\n"
    "ds_read_b128 v[240:243], v33 offset:40960\n"
    "v_mfma_f32_32x32x16_bf16 v[176:191], a[224:227], v[228:231], v[176:191]\n"
    "ds_read_b128 v[244:247], v33 offset:57344\n"
    "buffer_load_dwordx4 v20, s[20:23], s53 offen offset:1024 lds\n"
    "s_waitcnt vmcnt(17) lgkmcnt(2)\n"
    "v_mfma_f32_32x32x16_bf16 v[160:175], a[228:231], v[232:235], v[160:175]\n"
    "ds_read_b128 v[224:227], v34 offset:40960\n"
    "v_mfma_f32_32x32x16_bf16 v[176:191], a[228:231], v[236:239], v[176:191]\n"
    "ds_read_b128 v[228:231], v34 offset:57344\n"
    "buffer_load_dwordx4 v21, s[20:23], s53 offen offset:2048 lds\n"
    "s_waitcnt vmcnt(17) lgkmcnt(2)\n"
    "v_mfma_f32_32x32x16_bf16 v[160:175], a[232:235], v[240:243], v[160:175]\n"
    "ds_read_b128 v[232:235], v35 offset:32768\n"
    "v_mfma_f32_32x32x16_bf16 v[176:191], a[232:235], v[244:247], v[176:191]\n"
    "ds_read_b128 v[236:239], v35 offset:49152\n"
    "buffer_load_dwordx4 v22, s[20:23], s53 offen offset:3072 lds\n"
    "s_add_u32 m0, s72, 0\n"
    "s_waitcnt vmcnt(17) lgkmcnt(2)\n"
    "v_mfma_f32_32x32x16_bf16 v[160:175], a[236:239], v[224:227], v[160:175]\n"
    "ds_read_b128 v[240:243], v36 offset:32768\n"
    "v_mfma_f32_32x32x16_bf16 v[176:191], a[236:239], v[228:231], v[176:191]\n"
    "ds_read_b128 v[244:247], v36 offset:49152\n"
    "buffer_load_dwordx4 v19, s[20:23], s51 offen offset:0 lds\n"
    "s_waitcnt vmcnt(17) lgkmcnt(2)\n"
    "v_mfma_f32_32x32x16_bf16 v[160:175], a[240:243], v[232:235], v[160:175]\n"
    "ds_read_b128 v[224:227], v35 offset:40960\n"
    "v_mfma_f32_32x32x16_bf16 v[176:191], a[240:243], v[236:239], v[176:191]\n"
    "ds_read_b128 v[228:231], v35 offset:57344\n"
    "buffer_load_dwordx4 v20, s[20:23], s51 offen offset:1024 lds\n"
    "s_waitcnt vmcnt(17) lgkmcnt(2)\n"
    "v_mfma_f32_32x32x16_bf16 v[160:175], a[244:247], v[240:243], v[160:175]\n"
    "ds_read_b128 v[232:235], v36 offset:40960\n"
    "v_mfma_f32_32x32x16_bf16 v[176:191], a[244:247], v[244:247], v[176:191]\n"
    "ds_read_b128 v[236:239], v36 offset:57344\n"
    "buffer_load_dwordx4 v21, s[20:23], s51 offen offset:2048 lds\n"
    "s_waitcnt vmcnt(17) lgkmcnt(2)\n"
    "v_mfma_f32_32x32x16_bf16 v[160:175], a[248:251], v[224:227], v[160:175]\n"
    "ds_read_b64_tr_b16 v[240:241], v17 offset:12288\n"
    "ds_read_b64_tr_b16 v[242:243], v17 offset:13312\n"
    "v_mfma_f32_32x32x16_bf16 v[176:191], a[248:251], v[228:231], v[176:191]\n"
    "ds_read_b64_tr_b16 v[244:245], v18 offset:12288\n"
    "ds_read_b64_tr_b16 v[246:247], v18 offset:13312\n"
    "buffer_load_dwordx4 v22, s[20:23], s51 offen offset:3072 lds\n"
    "s_add_u32 m0, s72, 16384\n"
    "s_waitcnt vmcnt(17) lgkmcnt(4)\n"
    "v_mfma_f32_32x32x16_bf16 v[160:175], a[252:255], v[232:235], v[160:175]\n"
    "ds_read_b64_tr_b16 v[224:225], v17 offset:14336\n"
    "ds_read_b64_tr_b16 v[226:227], v17 offset:15360\n"
    "v_mfma_f32_32x32x16_bf16 v[176:191], a[252:255], v[236:239], v[176:191]\n"
    "ds_read_b64_tr_b16 v[228:229], v18 offset:14336\n"
    "ds_read_b64_tr_b16 v[230:231], v18 offset:15360\n"
    "buffer_load_dwordx4 v19, s[20:23], s52 offen offset:0 lds\n"
    "s_waitcnt vmcnt(14) lgkmcnt(4)\n"
    "v_mfma_f32_32x32x16_bf16 v[160:175], a[136:139], v[240:243], v[160:175]\n"
    "ds_read_b64_tr_b16 v[232:233], v17 offset:20480\n"
    "ds_read_b64_tr_b16 v[234:235], v17 offset:21504\n"
    "v_mfma_f32_32x32x16_bf16 v[176:191], a[136:139], v[244:247], v[176:191]\n"
    "ds_read_b64_tr_b16 v[236:237], v18 offset:20480\n"
    "ds_read_b64_tr_b16 v[238:239], v18 offset:21504\n"
    "buffer_load_dwordx4 v20, s[20:23], s52 offen offset:1024 lds\n"
    "s_waitcnt vmcnt(14) lgkmcnt(4)\n"
    "v_mfma_f32_32x32x16_bf16 v[160:175], a[140:143], v[224:227], v[160:175]\n"
    "ds_read_b64_tr_b16 v[240:241], v17 offset:22528\n"
    "ds_read_b64_tr_b16 v[242:243], v17 offset:23552\n"
    "v_mfma_f32_32x32x16_bf16 v[176:191], a[140:143], v[228:231], v[176:191]\n"
    "ds_read_b64_tr_b16 v[244:245], v18 offset:22528\n"
    "ds_read_b64_tr_b16 v[246:247], v18 offset:23552\n"
    "buffer_load_dwordx4 v21, s[20:23], s52 offen offset:2048 lds\n"
    "s_waitcnt vmcnt(14) lgkmcnt(4)\n"
    "v_mfma_f32_32x32x16_bf16 v[160:175], a[152:155], v[232:235], v[160:175]\n"
    "ds_read_b64_tr_b16 v[224:225], v17 offset:28672\n"
    "ds_read_b64_tr_b16 v[226:227], v17 offset:29696\n"
    "v_mfma_f32_32x32x16_bf16 v[176:191], a[152:155], v[236:239], v[176:191]\n"
    "ds_read_b64_tr_b16 v[228:229], v18 offset:28672\n"
    "ds_read_b64_tr_b16 v[230:231], v18 offset:29696\n"
    "buffer_load_dwordx4 v22, s[20:23], s52 offen offset:3072 lds\n"
    "s_waitcnt vmcnt(14) lgkmcnt(4)\n"
    "v_mfma_f32_32x32x16_bf16 v[160:175], a[156:159], v[240:243], v[160:175]\n"
    "ds_read_b64_tr_b16 v[232:233], v17 offset:30720\n"
    "ds_read_b64_tr_b16 v[234:235], v17 offset:31744\n"
    "v_mfma_f32_32x32x16_bf16 v[176:191], a[156:159], v[244:247], v[176:191]\n"
    "ds_read_b64_tr_b16 v[236:237], v18 offset:30720\n"
    "ds_read_b64_tr_b16 v[238:239], v18 offset:31744\n"
    "s_cmp_lg_u32 s63, 3\n"
    "s_cbranch_scc1 .Lsoa\n"
    "buffer_load_dwordx4 v[208:211], v42, s[32:35], s45 offen offset:0 sc1\n"
    "buffer_load_dwordx4 v[212:215], v42, s[32:35], s45 offen offset:1024 sc1\n"
    "buffer_load_dwordx4 v[216:219], v42, s[32:35], s45 offen offset:2048 sc1\n"
    "buffer_load_dwordx4 v[220:223], v42, s[32:35], s45 offen offset:3072 sc1\n"
    "buffer_load_dword v11, v52, s[32:35], s94 offen sc1\n"
    ".Lsoa:\n"
    "s_waitcnt vmcnt(8)\n"
    "s_barrier\n"
    "s_waitcnt lgkmcnt(4)\n"
    "v_mfma_f32_32x32x16_bf16 v[160:175], a[168:171], v[224:227], v[160:175]\n"
    "ds_read_b64_tr_b16 v[240:241], v27 offset:0\n"
    "ds_read_b64_tr_b16 v[242:243], v27 offset:1024\n"
    "v_mfma_f32_32x32x16_bf16 v[176:191], a[168:171], v[228:231], v[176:191]\n"
    "ds_read_b64_tr_b16 v[244:245], v28 offset:0\n"
    "ds_read_b64_tr_b16 v[246:247], v28 offset:1024\n"
    "buffer_load_dwordx4 v[128:131], v42, s[32:35], s46 offen offset:0 sc1\n"
    "s_waitcnt lgkmcnt(4)\n"
    "v_mfma_f32_32x32x16_bf16 v[160:175], a[172:175], v[232:235], v[160:175]\n"
    "ds_read_b64_tr_b16 v[224:225], v27 offset:2048\n"
    "ds_read_b64_tr_b16 v[226:227], v27 offset:3072\n"
    "v_mfma_f32_32x32x16_bf16 v[176:191], a[172:175], v[236:239], v[176:191]\n"
    "ds_read_b64_tr_b16 v[228:229], v28 offset:2048\n"
    "ds_read_b64_tr_b16 v[230:231], v28 offset:3072\n"
    "buffer_load_dwordx4 v[132:135], v42, s[32:35], s46 offen offset:1024 sc1\n"
    "buffer_load_dwordx4 v[136:139], v42, s[32:35], s46 offen offset:2048 sc1\n"
    "buffer_load_dwordx4 v[140:143], v42, s[32:35], s46 offen offset:3072 sc1\n"
    "buffer_load_dword v12, v52, s[32:35], s95 offen sc1\n"
    "buffer_load_dwordx4 v[112:115], v42, s[32:35], s47 offen offset:0 sc1\n"
    "buffer_load_dwordx4 v[116:119], v42, s[32:35], s47 offen offset:1024 sc1\n"
    "buffer_load_dwordx4 v[120:123], v42, s[32:35], s47 offen offset:2048 sc1\n"
    "buffer_load_dwordx4 v[124:127], v42, s[32:35], s47 offen offset:3072 sc1\n"
    "buffer_load_dword v13, v52, s[32:35], s96 offen sc1\n"
    "s_cmp_lg_u32 s63, 3\n"
    "s_cbranch_scc1 .Lsob\n"
    "s_waitcnt vmcnt(10)\n"
    "v_sub_u32 v67, s86, v11\n"
    "v_add3_u32 v67, v208, v209, v67\n"
    "v_add3_u32 v67, v210, v211, v67\n"
    "v_add3_u32 v67, v212, v213, v67\n"
    "v_add3_u32 v67, v214, v215, v67\n"
    "v_add3_u32 v67, v216, v217, v67\n"
    "v_add3_u32 v67, v218, v219, v67\n"
    "v_add3_u32 v67, v220, v221, v67\n"
    "v_add3_u32 v67, v222, v223, v67\n"
    "v_cmp_ne_u32 vcc, 0, v67\n"
    "s_nop 0\n"
    "s_cmp_eq_u64 vcc, 0\n"
    "s_cbranch_scc0 .Lsl5\n"
    ".Lok5:\n"
    ".Lsob:\n"
    "s_waitcnt lgkmcnt(4)\n"
    "v_mfma_f32_32x32x16_bf16 v[160:175], v[208:211], v[240:243], v[160:175]\n"
    "ds_read_b64_tr_b16 v[232:233], v27 offset:4096\n"
    "ds_read_b64_tr_b16 v[234:235], v27 offset:5120\n"
    "s_lshl2_add_u32 s12, s61, s60\n"
    "v_mfma_f32_32x32x16_bf16 v[176:191], v[208:211], v[244:247], v[176:191]\n"
    "ds_read_b64_tr_b16 v[236:237], v28 offset:4096\n"
    "ds_read_b64_tr_b16 v[238:239], v28 offset:5120\n"
    "s_lshl_b32 s12, s12, 15\n"
    "s_lshl2_add_u32 s74, s63, s64\n"
    "s_barrier\n"
    "s_waitcnt lgkmcnt(4)\n"
    "v_mfma_f32_32x32x16_bf16 v[160:175], v[212:215], v[224:227], v[160:175]\n"
    "ds_read_b64_tr_b16 v[240:241], v27 offset:6144\n"
    "ds_read_b64_tr_b16 v[242:243], v27 offset:7168\n"
    "s_lshl_b32 s74, s74, 6\n"
    "v_mfma_f32_32x32x16_bf16 v[176:191], v[212:215], v[228:231], v[176:191]\n"
    "ds_read_b64_tr_b16 v[244:245], v28 offset:6144\n"
    "ds_read_b64_tr_b16 v[246:247], v28 offset:7168\n"
    "s_add_u32 s74, s12, s74\n"
    "s_lshl_b32 s12, s74, 1\n"
    "s_waitcnt lgkmcnt(4)\n"
    "v_mfma_f32_32x32x16_bf16 v[160:175], v[216:219], v[232:235], v[160:175]\n"
    "ds_read_b64_tr_b16 v[224:225], v27 offset:8192\n"
    "ds_read_b64_tr_b16 v[226:227], v27 offset:9216\n"
    "v_lshrrev_b32 v37, 2, v5\n"
    "v_mfma_f32_32x32x16_bf16 v[176:191], v[216:219], v[236:239], v[176:191]\n"
    "ds_read_b64_tr_b16 v[228:229], v28 offset:8192\n"
    "ds_read_b64_tr_b16 v[230:231], v28 offset:9216\n"
    "v_lshlrev_b32 v37, 10, v37\n"
    "v_and_or_b32 v37, v42, 48, v37\n"
    "s_waitcnt lgkmcnt(4)\n"
    "v_mfma_f32_32x32x16_bf16 v[160:175], v[220:223], v[240:243], v[160:175]\n"
    "ds_read_b64_tr_b16 v[232:233], v27 offset:10240\n"
    "ds_read_b64_tr_b16 v[234:235], v27 offset:11264\n"
    "s_lshr_b32 s14, s72, 1\n"
    "v_mfma_f32_32x32x16_bf16 v[176:191], v[220:223], v[244:247], v[176:191]\n"
    "ds_read_b64_tr_b16 v[236:237], v28 offset:10240\n"
    "ds_read_b64_tr_b16 v[238:239], v28 offset:11264\n"
    "s_add_u32 m0, s14, 155648\n"
    "s_nop 0\n"
    "buffer_load_dwordx4 v37, s[4:7], s74 offen lds\n"
    "s_waitcnt vmcnt(6)\n"
    "v_sub_u32 v67, s86, v12\n"
    "v_add3_u32 v67, v128, v129, v67\n"
    "v_add3_u32 v67, v130, v131, v67\n"
    "v_add3_u32 v67, v132, v133, v67\n"
    "v_add3_u32 v67, v134, v135, v67\n"
    "v_add3_u32 v67, v136, v137, v67\n"
    "v_add3_u32 v67, v138, v139, v67\n"
    "v_add3_u32 v67, v140, v141, v67\n"
    "v_add3_u32 v67, v142, v143, v67\n"
    "s_waitcnt vmcnt(1)\n"
    "v_sub_u32 v4, s86, v13\n"
    "v_add3_u32 v4, v112, v113, v4\n"
    "v_add3_u32 v4, v114, v115, v4\n"
    "v_add3_u32 v4, v116, v117, v4\n"
    "v_add3_u32 v4, v118, v119, v4\n"
    "v_add3_u32 v4, v120, v121, v4\n"
    "v_add3_u32 v4, v122, v123, v4\n"
    "v_add3_u32 v4, v124, v125, v4\n"
    "v_add3_u32 v4, v126, v127, v4\n"
    "v_or_b32 v67, v67, v4\n"
    "v_cmp_ne_u32 vcc, 0, v67\n"
    "s_nop 0\n"
    "s_cmp_eq_u64 vcc, 0\n"
    "s_cbranch_scc0 .Lsl6\n"
    ".Lok6:\n"
    "s_waitcnt lgkmcnt(4)\n"
    "v_mfma_f32_32x32x16_bf16 v[160:175], v[128:131], v[224:227], v[160:175]\n"
    "ds_read_b64_tr_b16 v[240:241], v27 offset:12288\n"
    "ds_read_b64_tr_b16 v[242:243], v27 offset:13312\n"
    "s_add_u32 s74, s74, 15360\n"
    "v_mfma_f32_32x32x16_bf16 v[176:191], v[128:131], v[228:231], v[176:191]\n"
    "ds_read_b64_tr_b16 v[244:245], v28 offset:12288\n"
    "ds_read_b64_tr_b16 v[246:247], v28 offset:13312\n"
    "buffer_load_dwordx4 v37, s[4:7], s74 offen offset:1024 lds\n"
    "v_lshlrev_b32 v37, 15, v8\n"
    "s_waitcnt lgkmcnt(4)\n"
    "v_mfma_f32_32x32x16_bf16 v[160:175], v[132:135], v[232:235], v[160:175]\n"
    "ds_read_b64_tr_b16 v[224:225], v27 offset:14336\n"
    "ds_read_b64_tr_b16 v[226:227], v27 offset:15360\n"
    "v_lshl_add_u32 v37, v7, 2, v37\n"
    "v_mfma_f32_32x32x16_bf16 v[176:191], v[132:135], v[236:239], v[176:191]\n"
    "ds_read_b64_tr_b16 v[228:229], v28 offset:14336\n"
    "ds_read_b64_tr_b16 v[230:231], v28 offset:15360\n"
    "s_add_u32 m0, s72, 49152\n"
    "s_nop 0\n"
    "buffer_load_dwordx4 v19, s[20:23], s79 offen offset:0 lds\n"
    "s_waitcnt lgkmcnt(4)\n"
    "v_mfma_f32_32x32x16_bf16 v[160:175], v[136:139], v[240:243], v[160:175]\n"
    "ds_read_b64_tr_b16 v[232:233], v15 offset:32768\n"
    "ds_read_b64_tr_b16 v[234:235], v15 offset:33792\n"
    "v_bfe_u32 v1, v5, 2, 2\n"
    "v_mfma_f32_32x32x16_bf16 v[176:191], v[136:139], v[244:247], v[176:191]\n"
    "ds_read_b64_tr_b16 v[236:237], v16 offset:32768\n"
    "ds_read_b64_tr_b16 v[238:239], v16 offset:33792\n"
    "buffer_load_dwordx4 v20, s[20:23], s79 offen offset:1024 lds\n"
    "v_lshl_add_u32 v1, v8, 4, v1\n"
    "s_waitcnt lgkmcnt(4)\n"
    "v_mfma_f32_32x32x16_bf16 v[160:175], v[140:143], v[224:227], v[160:175]\n"
    "ds_read_b64_tr_b16 v[240:241], v15 offset:34816\n"
    "ds_read_b64_tr_b16 v[242:243], v15 offset:35840\n"
    "v_lshlrev_b32 v1, 6, v1\n"
    "v_mfma_f32_32x32x16_bf16 v[176:191], v[140:143], v[228:231], v[176:191]\n"
    "ds_read_b64_tr_b16 v[244:245], v16 offset:34816\n"
    "ds_read_b64_tr_b16 v[246:247], v16 offset:35840\n"
    "buffer_load_dwordx4 v21, s[20:23], s79 offen offset:2048 lds\n"
    "v_bfe_u32 v2, v5, 4, 1\n"
    "s_waitcnt lgkmcnt(4)\n"
    "v_mfma_f32_32x32x16_bf16 v[160:175], v[112:115], v[232:235], v[160:175]\n"
    "ds_read_b64_tr_b16 v[224:225], v15 offset:36864\n"
    "ds_read_b64_tr_b16 v[226:227], v15 offset:37888\n"
    "v_lshl_add_u32 v1, v2, 5, v1\n"
    "v_mfma_f32_32x32x16_bf16 v[176:191], v[112:115], v[236:239], v[176:191]\n"
    "ds_read_b64_tr_b16 v[228:229], v16 offset:36864\n"
    "ds_read_b64_tr_b16 v[230:231], v16 offset:37888\n"
    "buffer_load_dwordx4 v22, s[20:23], s79 offen offset:3072 lds\n"
    "s_xor_b32 s74, s64, 1\n"
    "s_waitcnt lgkmcnt(4)\n"
    "v_mfma_f32_32x32x16_bf16 v[160:175], v[116:119], v[240:243], v[160:175]\n"
    "ds_read_b64_tr_b16 v[232:233], v15 offset:38912\n"
    "ds_read_b64_tr_b16 v[234:235], v15 offset:39936\n"
    "s_lshl_b32 s74, s74, 15\n"
    "v_mfma_f32_32x32x16_bf16 v[176:191], v[116:119], v[244:247], v[176:191]\n"
    "ds_read_b64_tr_b16 v[236:237], v16 offset:38912\n"
    "ds_read_b64_tr_b16 v[238:239], v16 offset:39936\n"
    "s_add_u32 m0, s72, 65536\n"
    "s_add_u32 s74, s74, s80\n"
    "s_waitcnt vmcnt(0)\n"
    "s_barrier\n"
    "s_waitcnt lgkmcnt(4)\n"
    "v_mfma_f32_32x32x16_bf16 v[160:175], v[120:123], v[224:227], v[160:175]\n"
    "ds_read_b64_tr_b16 v[240:241], v16 offset:0\n"
    "ds_read_b64_tr_b16 v[242:243], v16 offset:1024\n"
    "v_and_b32 v2, 3, v5\n"
    "v_mfma_f32_32x32x16_bf16 v[176:191], v[120:123], v[228:231], v[176:191]\n"
    "ds_read_b64_tr_b16 v[244:245], v16 offset:2048\n"
    "ds_read_b64_tr_b16 v[246:247], v16 offset:3072\n"
    "buffer_load_dwordx4 v23, s[24:27], s74 offen offset:0 lds\n"
    "v_lshl_add_u32 v1, v2, 3, v1\n"
    "s_waitcnt lgkmcnt(4)\n"
    "v_mfma_f32_32x32x16_bf16 v[160:175], v[124:127], v[232:235], v[160:175]\n"
    "ds_read_b64_tr_b16 v[224:225], v16 offset:4096\n"
    "ds_read_b64_tr_b16 v[226:227], v16 offset:5120\n"
    "s_lshr_b32 s68, s72, 1\n"
    "v_mfma_f32_32x32x16_bf16 v[176:191], v[124:127], v[236:239], v[176:191]\n"
    "ds_read_b64_tr_b16 v[228:229], v16 offset:6144\n"
    "ds_read_b64_tr_b16 v[230:231], v16 offset:7168\n"
    "buffer_load_dwordx4 v24, s[24:27], s74 offen offset:1024 lds\n"
    "s_add_u32 s68, s68, 155648\n"
    "s_waitcnt lgkmcnt(4)\n"
    "v_mfma_f32_32x32x16_bf16 v[96:111], v[80:83], v[240:243], 0\n"
    "ds_read_b64_tr_b16 v[232:233], v16 offset:8192\n"
    "ds_read_b64_tr_b16 v[234:235], v16 offset:9216\n"
    "v_add_u32 v1, s68, v1\n"
    "v_mfma_f32_32x32x16_bf16 v[96:111], v[84:87], v[244:247], v[96:111]\n"
    "ds_read_b64_tr_b16 v[236:237], v16 offset:10240\n"
    "ds_read_b64_tr_b16 v[238:239], v16 offset:11264\n"
    "buffer_load_dwordx4 v25, s[24:27], s74 offen offset:2048 lds\n"
    "ds_read_b64_tr_b16 v[248:249], v1 offset:0\n"
    "s_waitcnt lgkmcnt(5)\n"
    "v_mfma_f32_32x32x16_bf16 v[96:111], v[88:91], v[224:227], v[96:111]\n"
    "ds_read_b64_tr_b16 v[240:241], v16 offset:12288\n"
    "ds_read_b64_tr_b16 v[242:243], v16 offset:13312\n"
    "s_xor_b32 s68, s64, 2\n"
    "v_mfma_f32_32x32x16_bf16 v[96:111], v[92:95], v[228:231], v[96:111]\n"
    "ds_read_b64_tr_b16 v[244:245], v16 offset:14336\n"
    "ds_read_b64_tr_b16 v[246:247], v16 offset:15360\n"
    "buffer_load_dwordx4 v26, s[24:27], s74 offen offset:3072 lds\n"
    "s_xor_b32 s74, s64, 0\n"
    "s_waitcnt lgkmcnt(5)\n"
    "v_mfma_f32_32x32x16_bf16 v[96:111], a[128:131], v[232:235], v[96:111]\n"
    "ds_read_b64_tr_b16 v[224:225], v16 offset:16384\n"
    "ds_read_b64_tr_b16 v[226:227], v16 offset:17408\n"
    "s_lshl_b32 s74, s74, 15\n"
    "v_mfma_f32_32x32x16_bf16 v[96:111], a[132:135], v[236:239], v[96:111]\n"
    "ds_read_b64_tr_b16 v[228:229], v16 offset:18432\n"
    "ds_read_b64_tr_b16 v[230:231], v16 offset:19456\n"
    "s_add_u32 m0, s72, 81920\n"
    "s_add_u32 s74, s74, s80\n"
    "s_waitcnt lgkmcnt(4)\n"
    "v_mfma_f32_32x32x16_bf16 v[96:111], a[136:139], v[240:243], v[96:111]\n"
    "ds_read_b64_tr_b16 v[232:233], v16 offset:20480\n"
    "ds_read_b64_tr_b16 v[234:235], v16 offset:21504\n"
    "s_lshl2_add_u32 s68, s68, s64\n"
    "v_mfma_f32_32x32x16_bf16 v[96:111], a[140:143], v[244:247], v[96:111]\n"
    "ds_read_b64_tr_b16 v[236:237], v16 offset:22528\n"
    "ds_read_b64_tr_b16 v[238:239], v16 offset:23552\n"
    "buffer_load_dwordx4 v23, s[24:27], s74 offen offset:0 lds\n"
    "s_lshl_b32 s68, s68, 14\n"
    "s_waitcnt lgkmcnt(4)\n"
    "v_mfma_f32_32x32x16_bf16 v[96:111], a[144:147], v[224:227], v[96:111]\n"
    "ds_read_b64_tr_b16 v[240:241], v16 offset:24576\n"
    "ds_read_b64_tr_b16 v[242:243], v16 offset:25600\n"
    "s_add_u32 s68, s68, s13\n"
    "v_mfma_f32_32x32x16_bf16 v[96:111], a[148:151], v[228:231], v[96:111]\n"
    "ds_read_b64_tr_b16 v[244:245], v16 offset:26624\n"
    "ds_read_b64_tr_b16 v[246:247], v16 offset:27648\n"
    "buffer_load_dwordx4 v24, s[24:27], s74 offen offset:1024 lds\n"
    "v_cvt_pk_bf16_f32 v160, v160, v161\n"
    "s_waitcnt lgkmcnt(4)\n"
    "v_mfma_f32_32x32x16_bf16 v[96:111], a[152:155], v[232:235], v[96:111]\n"
    "ds_read_b64_tr_b16 v[224:225], v16 offset:28672\n"
    "ds_read_b64_tr_b16 v[226:227], v16 offset:29696\n"
    "v_cvt_pk_bf16_f32 v161, v162, v163\n"
    "v_mfma_f32_32x32x16_bf16 v[96:111], a[156:159], v[236:239], v[96:111]\n"
    "ds_read_b64_tr_b16 v[228:229], v16 offset:30720\n"
    "ds_read_b64_tr_b16 v[230:231], v16 offset:31744\n"
    "buffer_load_dwordx4 v25, s[24:27], s74 offen offset:2048 lds\n"
    "v_add3_u32 v2, v160, v161, s86\n"
    "s_waitcnt lgkmcnt(4)\n"
    "v_mfma_f32_32x32x16_bf16 v[96:111], a[160:163], v[240:243], v[96:111]\n"
    "ds_read_b64_tr_b16 v[232:233], v16 offset:40960\n"
    "ds_read_b64_tr_b16 v[234:235], v16 offset:41984\n"
    "v_cvt_pk_bf16_f32 v162, v164, v165\n"
    "v_mfma_f32_32x32x16_bf16 v[96:111], a[164:167], v[244:247], v[96:111]\n"
    "ds_read_b64_tr_b16 v[236:237], v16 offset:43008\n"
    "ds_read_b64_tr_b16 v[238:239], v16 offset:44032\n"
    "buffer_load_dwordx4 v26, s[24:27], s74 offen offset:3072 lds\n"
    "v_cvt_pk_bf16_f32 v163, v166, v167\n"
    "s_waitcnt lgkmcnt(4)\n"
    "v_mfma_f32_32x32x16_bf16 v[96:111], a[168:171], v[224:227], v[96:111]\n"
    "ds_read_b64_tr_b16 v[240:241], v16 offset:45056\n"
    "ds_read_b64_tr_b16 v[242:243], v16 offset:46080\n"
    "v_add3_u32 v2, v162, v163, v2\n"
    "v_mfma_f32_32x32x16_bf16 v[96:111], a[172:175], v[228:231], v[96:111]\n"
    "ds_read_b64_tr_b16 v[244:245], v16 offset:47104\n"
    "ds_read_b64_tr_b16 v[246:247], v16 offset:48128\n"
    "buffer_store_dwordx4 v[160:163], v42, s[32:35], s68 offen offset:0 sc1\n"
    "v_cvt_pk_bf16_f32 v164, v168, v169\n"
    "s_waitcnt lgkmcnt(4)\n"
    "v_mfma_f32_32x32x16_bf16 v[96:111], v[208:211], v[232:235], v[96:111]\n"
    "ds_read_b64_tr_b16 v[224:225], v16 offset:49152\n"
    "ds_read_b64_tr_b16 v[226:227], v16 offset:50176\n"
    "v_cvt_pk_bf16_f32 v165, v170, v171\n"
    "v_mfma_f32_32x32x16_bf16 v[96:111], v[212:215], v[236:239], v[96:111]\n"
    "ds_read_b64_tr_b16 v[228:229], v16 offset:51200\n"
    "ds_read_b64_tr_b16 v[230:231], v16 offset:52224\n"
    "v_add3_u32 v2, v164, v165, v2\n"
    "v_cvt_pk_bf16_f32 v166, v172, v173\n"
    "s_waitcnt lgkmcnt(4)\n"
    "v_mfma_f32_32x32x16_bf16 v[96:111], v[216:219], v[240:243], v[96:111]\n"
    "ds_read_b64_tr_b16 v[232:233], v16 offset:53248\n"
    "ds_read_b64_tr_b16 v[234:235], v16 offset:54272\n"
    "v_cvt_pk_bf16_f32 v167, v174, v175\n"
    "v_mfma_f32_32x32x16_bf16 v[96:111], v[220:223], v[244:247], v[96:111]\n"
    "ds_read_b64_tr_b16 v[236:237], v16 offset:55296\n"
    "ds_read_b64_tr_b16 v[238:239], v16 offset:56320\n"
    "v_add3_u32 v2, v166, v167, v2\n"
    "buffer_store_dwordx4 v[164:167], v42, s[32:35], s68 offen offset:1024 sc1\n"
    "s_waitcnt lgkmcnt(4)\n"
    "v_mfma_f32_32x32x16_bf16 v[96:111], v[128:131], v[224:227], v[96:111]\n"
    "ds_read_b64_tr_b16 v[240:241], v16 offset:57344\n"
    "ds_read_b64_tr_b16 v[242:243], v16 offset:58368\n"
    "buffer_store_dword v2, v52, s[32:35], s68 offen offset:2048 sc1\n"
    "v_mfma_f32_32x32x16_bf16 v[96:111], v[132:135], v[228:231], v[96:111]\n"
    "ds_read_b64_tr_b16 v[244:245], v16 offset:59392\n"
    "ds_read_b64_tr_b16 v[246:247], v16 offset:60416\n"
    "s_xor_b32 s68, s64, 3\n"
    "s_lshl2_add_u32 s68, s68, s64\n"
    "s_waitcnt lgkmcnt(4)\n"
    "v_mfma_f32_32x32x16_bf16 v[96:111], v[136:139], v[232:235], v[96:111]\n"
    "ds_read_b64_tr_b16 v[224:225], v16 offset:61440\n"
    "ds_read_b64_tr_b16 v[226:227], v16 offset:62464\n"
    "s_lshl_b32 s68, s68, 14\n"
    "v_mfma_f32_32x32x16_bf16 v[96:111], v[140:143], v[236:239], v[96:111]\n"
    "ds_read_b64_tr_b16 v[228:229], v16 offset:63488\n"
    "ds_read_b64_tr_b16 v[230:231], v16 offset:64512\n"
    "s_add_u32 s68, s68, s13\n"
    "v_cvt_pk_bf16_f32 v176, v176, v177\n"
    "s_waitcnt vmcnt(3)\n"
    "s_barrier\n"
    "s_waitcnt lgkmcnt(4)\n"
    "v_mfma_f32_32x32x16_bf16 v[96:111], v[112:115], v[240:243], v[96:111]\n"
    "ds_read_b128 v[232:235], v29 offset:0\n"
    "v_cvt_pk_bf16_f32 v177, v178, v179\n"
    "v_add3_u32 v2, v176, v177, s86\n"
    "v_mfma_f32_32x32x16_bf16 v[96:111], v[116:119], v[244:247], v[96:111]\n"
    "ds_read_b128 v[236:239], v30 offset:0\n"
    "v_cvt_pk_bf16_f32 v178, v180, v181\n"
    "v_cvt_pk_bf16_f32 v179, v182, v183\n"
    "v_add3_u32 v2, v178, v179, v2\n"
    "s_waitcnt lgkmcnt(2)\n"
    "v_mfma_f32_32x32x16_bf16 v[96:111], v[120:123], v[224:227], v[96:111]\n"
    "ds_read_b128 v[240:243], v29 offset:8192\n"
    "buffer_store_dwordx4 v[176:179], v42, s[32:35], s68 offen offset:0 sc1\n"
    "v_cvt_pk_bf16_f32 v180, v184, v185\n"
    "v_mfma_f32_32x32x16_bf16 v[96:111], v[124:127], v[228:231], v[96:111]\n"
    "ds_read_b128 v[244:247], v30 offset:8192\n"
    "v_cvt_pk_bf16_f32 v181, v186, v187\n"
    "v_add3_u32 v2, v180, v181, v2\n"
    "v_cvt_pk_bf16_f32 v182, v188, v189\n"
    "s_waitcnt lgkmcnt(2)\n"
    "v_mfma_f32_32x32x16_bf16 v[96:111], a[176:179], v[232:235], v[96:111]\n"
    "ds_read_b128 v[224:227], v31 offset:0\n"
    "v_cvt_pk_bf16_f32 v183, v190, v191\n"
    "v_add3_u32 v2, v182, v183, v2\n"
    "v_mfma_f32_32x32x16_bf16 v[96:111], a[180:183], v[236:239], v[96:111]\n"
    "ds_read_b128 v[228:231], v32 offset:0\n"
    "buffer_store_dwordx4 v[180:183], v42, s[32:35], s68 offen offset:1024 sc1\n"
    "buffer_store_dword v2, v52, s[32:35], s68 offen offset:2048 sc1\n"
    "ds_read_b64_tr_b16 v[250:251], v1 offset:256\n"
    "s_waitcnt lgkmcnt(3)\n"
    "v_mfma_f32_32x32x16_bf16 v[96:111], a[184:187], v[240:243], v[96:111]\n"
    "ds_read_b128 v[232:235], v31 offset:8192\n"
    "ds_read_b64_tr_b16 v[252:253], v1 offset:512\n"
    "ds_read_b64_tr_b16 v[254:255], v1 offset:768\n"
    "v_mfma_f32_32x32x16_bf16 v[96:111], a[188:191], v[244:247], v[96:111]\n"
    "ds_read_b128 v[236:239], v32 offset:8192\n"
    "ds_read_b128 v[192:195], v44 offset:0\n"
    "ds_read_b128 v[144:147], v44 offset:512\n"
    "ds_read_b128 v[196:199], v44 offset:16\n"
    "s_waitcnt lgkmcnt(8)\n"
    "v_mfma_f32_32x32x16_bf16 v[96:111], a[208:211], v[224:227], v[96:111]\n"
    "ds_read_b128 v[240:243], v33 offset:0\n"
    "ds_read_b128 v[148:151], v44 offset:528\n"
    "ds_read_b128 v[200:203], v44 offset:32\n"
    "v_mfma_f32_32x32x16_bf16 v[96:111], a[212:215], v[228:231], v[96:111]\n"
    "ds_read_b128 v[244:247], v34 offset:0\n"
    "ds_read_b128 v[152:155], v44 offset:544\n"
    "ds_read_b128 v[204:207], v44 offset:48\n"
    "ds_read_b128 v[156:159], v44 offset:560\n"
    "s_waitcnt lgkmcnt(10)\n"
    "v_mfma_f32_32x32x16_bf16 v[96:111], a[216:219], v[232:235], v[96:111]\n"
    "ds_read_b128 v[224:227], v33 offset:8192\n"
    "v_lshlrev_b32 v9, 16, v9\n"
    "v_lshlrev_b32 v10, 16, v10\n"
    "v_mfma_f32_32x32x16_bf16 v[96:111], a[220:223], v[236:239], v[96:111]\n"
    "ds_read_b128 v[228:231], v34 offset:8192\n"
    "v_lshlrev_b32 v1, 16, v248\n"
    "s_waitcnt lgkmcnt(11)\n"
    "v_mul_f32 v192, v192, v9\n"
    "s_waitcnt lgkmcnt(10)\n"
    "v_sub_f32 v144, v1, v144\n"
    "s_waitcnt lgkmcnt(5)\n"
    "v_mfma_f32_32x32x16_bf16 v[96:111], a[224:227], v[240:243], v[96:111]\n"
    "ds_read_b128 v[232:235], v35 offset:0\n"
    "v_add_f32 v1, v1, v10\n"
    "v_fma_f32 v192, v192, v144, v1\n"
    "v_mfma_f32_32x32x16_bf16 v[96:111], a[228:231], v[244:247], v[96:111]\n"
    "ds_read_b128 v[236:239], v36 offset:0\n"
    "v_and_b32 v1, 0xffff0000, v248\n"
    "v_mul_f32 v193, v193, v9\n"
    "v_sub_f32 v145, v1, v145\n"
    "s_waitcnt lgkmcnt(2)\n"
    "v_mfma_f32_32x32x16_bf16 v[96:111], a[232:235], v[224:227], v[96:111]\n"
    "ds_read_b128 v[240:243], v35 offset:8192\n"
    "v_add_f32 v1, v1, v10\n"
    "v_fma_f32 v193, v193, v145, v1\n"
    "v_mfma_f32_32x32x16_bf16 v[96:111], a[236:239], v[228:231], v[96:111]\n"
    "ds_read_b128 v[244:247], v36 offset:8192\n"
    "v_lshlrev_b32 v1, 16, v249\n"
    "v_mul_f32 v194, v194, v9\n"
    "v_sub_f32 v146, v1, v146\n"
    "s_waitcnt lgkmcnt(2)\n"
    "v_mfma_f32_32x32x16_bf16 v[96:111], a[240:243], v[232:235], v[96:111]\n"
    "ds_read_b64_tr_b16 v[224:225], v15 offset:0\n"
    "ds_read_b64_tr_b16 v[226:227], v15 offset:1024\n"
    "v_add_f32 v1, v1, v10\n"
    "v_mfma_f32_32x32x16_bf16 v[96:111], a[244:247], v[236:239], v[96:111]\n"
    "ds_read_b64_tr_b16 v[228:229], v15 offset:2048\n"
    "ds_read_b64_tr_b16 v[230:231], v15 offset:3072\n"
    "v_fma_f32 v194, v194, v146, v1\n"
    "v_and_b32 v1, 0xffff0000, v249\n"
    "s_waitcnt lgkmcnt(4)\n"
    "v_mfma_f32_32x32x16_bf16 v[96:111], a[248:251], v[240:243], v[96:111]\n"
    "ds_read_b64_tr_b16 v[232:233], v15 offset:4096\n"
    "ds_read_b64_tr_b16 v[234:235], v15 offset:5120\n"
    "v_mul_f32 v195, v195, v9\n"
    "v_mfma_f32_32x32x16_bf16 v[96:111], a[252:255], v[244:247], v[96:111]\n"
    "ds_read_b64_tr_b16 v[236:237], v15 offset:6144\n"
    "ds_read_b64_tr_b16 v[238:239], v15 offset:7168\n"
    "v_sub_f32 v147, v1, v147\n"
    "v_add_f32 v1, v1, v10\n"
    "v_fma_f32 v195, v195, v147, v1\n"
    "v_lshlrev_b32 v1, 16, v250\n"
    "v_mul_f32 v196, v196, v9\n"
    "v_sub_f32 v148, v1, v148\n"
    "v_add_f32 v1, v1, v10\n"
    "v_fma_f32 v196, v196, v148, v1\n"
    "v_and_b32 v1, 0xffff0000, v250\n"
    "v_mul_f32 v197, v197, v9\n"
    "v_sub_f32 v149, v1, v149\n"
    "v_add_f32 v1, v1, v10\n"
    "v_fma_f32 v197, v197, v149, v1\n"
    "v_lshlrev_b32 v1, 16, v251\n"
    "v_mul_f32 v198, v198, v9\n"
    "v_sub_f32 v150, v1, v150\n"
    "v_add_f32 v1, v1, v10\n"
    "v_fma_f32 v198, v198, v150, v1\n"
    "v_and_b32 v1, 0xffff0000, v251\n"
    "v_mul_f32 v199, v199, v9\n"
    "v_sub_f32 v151, v1, v151\n"
    "v_add_f32 v1, v1, v10\n"
    "v_fma_f32 v199, v199, v151, v1\n"
    "v_lshlrev_b32 v1, 16, v252\n"
    "v_mul_f32 v200, v200, v9\n"
    "v_sub_f32 v152, v1, v152\n"
    "v_add_f32 v1, v1, v10\n"
    "v_fma_f32 v200, v200, v152, v1\n"
    "v_and_b32 v1, 0xffff0000, v252\n"
    "v_mul_f32 v201, v201, v9\n"
    "v_sub_f32 v153, v1, v153\n"
    "v_add_f32 v1, v1, v10\n"
    "v_fma_f32 v201, v201, v153, v1\n"
    "v_lshlrev_b32 v1, 16, v253\n"
    "v_mul_f32 v202, v202, v9\n"
    "v_sub_f32 v154, v1, v154\n"
    "v_add_f32 v1, v1, v10\n"
    "v_fma_f32 v202, v202, v154, v1\n"
    "v_and_b32 v1, 0xffff0000, v253\n"
    "v_mul_f32 v203, v203, v9\n"
    "v_sub_f32 v155, v1, v155\n"
    "v_add_f32 v1, v1, v10\n"
    "v_fma_f32 v203, v203, v155, v1\n"
    "v_lshlrev_b32 v1, 16, v254\n"
    "v_mul_f32 v204, v204, v9\n"
    "v_sub_f32 v156, v1, v156\n"
    "v_add_f32 v1, v1, v10\n"
    "v_fma_f32 v204, v204, v156, v1\n"
    "v_and_b32 v1, 0xffff0000, v254\n"
    "v_mul_f32 v205, v205, v9\n"
    "v_sub_f32 v157, v1, v157\n"
    "v_add_f32 v1, v1, v10\n"
    "v_fma_f32 v205, v205, v157, v1\n"
    "v_lshlrev_b32 v1, 16, v255\n"
    "v_mul_f32 v206, v206, v9\n"
    "v_sub_f32 v158, v1, v158\n"
    "v_add_f32 v1, v1, v10\n"
    "v_fma_f32 v206, v206, v158, v1\n"
    "v_and_b32 v1, 0xffff0000, v255\n"
    "v_mul_f32 v207, v207, v9\n"
    "v_sub_f32 v159, v1, v159\n"
    "v_add_f32 v1, v1, v10\n"
    "v_fma_f32 v207, v207, v159, v1\n"
    "s_waitcnt lgkmcnt(4)\n"
    "v_mfma_f32_32x32x16_bf16 v[144:159], v[80:83], v[224:227], 0\n"
    "ds_read_b64_tr_b16 v[240:241], v15 offset:8192\n"
    "ds_read_b64_tr_b16 v[242:243], v15 offset:9216\n"
    "v_mfma_f32_32x32x16_bf16 v[144:159], v[84:87], v[228:231], v[144:159]\n"
    "ds_read_b64_tr_b16 v[244:245], v15 offset:10240\n"
    "ds_read_b64_tr_b16 v[246:247], v15 offset:11264\n"
    "s_waitcnt lgkmcnt(4)\n"
    "v_mfma_f32_32x32x16_bf16 v[144:159], v[88:91], v[232:235], v[144:159]\n"
    "ds_read_b64_tr_b16 v[224:225], v15 offset:12288\n"
    "ds_read_b64_tr_b16 v[226:227], v15 offset:13312\n"
    "s_xor_b32 s68, s64, 1\n"
    "v_mfma_f32_32x32x16_bf16 v[144:159], v[92:95], v[236:239], v[144:159]\n"
    "ds_read_b64_tr_b16 v[228:229], v15 offset:14336\n"
    "ds_read_b64_tr_b16 v[230:231], v15 offset:15360\n"
    "s_lshl2_add_u32 s68, s68, s64\n"
    "s_lshl_b32 s68, s68, 14\n"
    "s_waitcnt lgkmcnt(4)\n"
    "v_mfma_f32_32x32x16_bf16 v[144:159], a[128:131], v[240:243], v[144:159]\n"
    "ds_read_b64_tr_b16 v[232:233], v15 offset:16384\n"
    "ds_read_b64_tr_b16 v[234:235], v15 offset:17408\n"
    "s_add_u32 s68, s68, s13\n"
    "v_mfma_f32_32x32x16_bf16 v[144:159], a[132:135], v[244:247], v[144:159]\n"
    "ds_read_b64_tr_b16 v[236:237], v15 offset:18432\n"
    "ds_read_b64_tr_b16 v[238:239], v15 offset:19456\n"
    "v_cvt_pk_bf16_f32 v96, v96, v97\n"
    "v_cvt_pk_bf16_f32 v97, v98, v99\n"
    "s_waitcnt lgkmcnt(4)\n"
    "v_mfma_f32_32x32x16_bf16 v[144:159], a[136:139], v[224:227], v[144:159]\n"
    "ds_read_b64_tr_b16 v[240:241], v15 offset:20480\n"
    "ds_read_b64_tr_b16 v[242:243], v15 offset:21504\n"
    "v_add3_u32 v2, v96, v97, s86\n"
    "v_mfma_f32_32x32x16_bf16 v[144:159], a[140:143], v[228:231], v[144:159]\n"
    "ds_read_b64_tr_b16 v[244:245], v15 offset:22528\n"
    "ds_read_b64_tr_b16 v[246:247], v15 offset:23552\n"
    "v_cvt_pk_bf16_f32 v98, v100, v101\n"
    "v_cvt_pk_bf16_f32 v99, v102, v103\n"
    "s_waitcnt lgkmcnt(4)\n"
    "v_mfma_f32_32x32x16_bf16 v[144:159], a[144:147], v[232:235], v[144:159]\n"
    "ds_read_b64_tr_b16 v[224:225], v15 offset:24576\n"
    "ds_read_b64_tr_b16 v[226:227], v15 offset:25600\n"
    "v_add3_u32 v2, v98, v99, v2\n"
    "v_mfma_f32_32x32x16_bf16 v[144:159], a[148:151], v[236:239], v[144:159]\n"
    "ds_read_b64_tr_b16 v[228:229], v15 offset:26624\n"
    "ds_read_b64_tr_b16 v[230:231], v15 offset:27648\n"
    "buffer_store_dwordx4 v[96:99], v42, s[32:35], s68 offen offset:0 sc1\n"
    "v_cvt_pk_bf16_f32 v100, v104, v105\n"
    "v_cvt_pk_bf16_f32 v101, v106, v107\n"
    "v_add3_u32 v2, v100, v101, v2\n"
    "v_cvt_pk_bf16_f32 v102, v108, v109\n"
    "v_cvt_pk_bf16_f32 v103, v110, v111\n"
    "v_add3_u32 v2, v102, v103, v2\n"
    "buffer_store_dwordx4 v[100:103], v42, s[32:35], s68 offen offset:1024 sc1\n"
    "buffer_store_dword v2, v52, s[32:35], s68 offen offset:2048 sc1\n"
    "s_waitcnt lgkmcnt(4)\n"
    "v_mfma_f32_32x32x16_bf16 v[144:159], a[152:155], v[240:243], v[144:159]\n"
    "ds_read_b64_tr_b16 v[232:233], v15 offset:28672\n"
    "ds_read_b64_tr_b16 v[234:235], v15 offset:29696\n"
    "s_xor_b32 s14, s64, 2\n"
    "v_mfma_f32_32x32x16_bf16 v[144:159], a[156:159], v[244:247], v[144:159]\n"
    "ds_read_b64_tr_b16 v[236:237], v15 offset:30720\n"
    "ds_read_b64_tr_b16 v[238:239], v15 offset:31744\n"
    "s_lshl2_add_u32 s14, s64, s14\n"
    "s_lshl_b32 s14, s14, 14\n"
    "s_waitcnt lgkmcnt(4)\n"
    "v_mfma_f32_32x32x16_bf16 v[144:159], a[160:163], v[224:227], v[144:159]\n"
    "ds_read_b64_tr_b16 v[240:241], v15 offset:40960\n"
    "ds_read_b64_tr_b16 v[242:243], v15 offset:41984\n"
    "s_add_u32 s14, s14, s13\n"
    "v_mfma_f32_32x32x16_bf16 v[144:159], a[164:167], v[228:231], v[144:159]\n"
    "ds_read_b64_tr_b16 v[244:245], v15 offset:43008\n"
    "ds_read_b64_tr_b16 v[246:247], v15 offset:44032\n"
    "buffer_load_dwordx4 v[160:163], v42, s[32:35], s14 offen offset:0 sc1\n"
    "s_waitcnt lgkmcnt(4)\n"
    "v_mfma_f32_32x32x16_bf16 v[144:159], a[168:171], v[232:235], v[144:159]\n"
    "ds_read_b64_tr_b16 v[224:225], v15 offset:45056\n"
    "ds_read_b64_tr_b16 v[226:227], v15 offset:46080\n"
    "v_mfma_f32_32x32x16_bf16 v[144:159], a[172:175], v[236:239], v[144:159]\n"
    "ds_read_b64_tr_b16 v[228:229], v15 offset:47104\n"
    "ds_read_b64_tr_b16 v[230:231], v15 offset:48128\n"
    "buffer_load_dwordx4 v[164:167], v42, s[32:35], s14 offen offset:1024 sc1\n"
    "s_waitcnt lgkmcnt(4)\n"
    "v_mfma_f32_32x32x16_bf16 v[144:159], v[208:211], v[240:243], v[144:159]\n"
    "ds_read_b64_tr_b16 v[232:233], v15 offset:49152\n"
    "ds_read_b64_tr_b16 v[234:235], v15 offset:50176\n"
    "v_mfma_f32_32x32x16_bf16 v[144:159], v[212:215], v[244:247], v[144:159]\n"
    "ds_read_b64_tr_b16 v[236:237], v15 offset:51200\n"
    "ds_read_b64_tr_b16 v[238:239], v15 offset:52224\n"
    "buffer_load_dword v11, v52, s[32:35], s14 offen offset:2048 sc1\n"
    "s_xor_b32 s14, s64, 3\n"
    "s_waitcnt lgkmcnt(4)\n"
    "v_mfma_f32_32x32x16_bf16 v[144:159], v[216:219], v[224:227], v[144:159]\n"
    "ds_read_b64_tr_b16 v[240:241], v15 offset:53248\n"
    "ds_read_b64_tr_b16 v[242:243], v15 offset:54272\n"
    "s_lshl2_add_u32 s14, s64, s14\n"
    "v_mfma_f32_32x32x16_bf16 v[144:159], v[220:223], v[228:231], v[144:159]\n"
    "ds_read_b64_tr_b16 v[244:245], v15 offset:55296\n"
    "ds_read_b64_tr_b16 v[246:247], v15 offset:56320\n"
    "s_lshl_b32 s14, s14, 14\n"
    "s_add_u32 s14, s14, s13\n"
    "s_waitcnt lgkmcnt(4)\n"
    "v_mfma_f32_32x32x16_bf16 v[144:159], v[128:131], v[232:235], v[144:159]\n"
    "ds_read_b64_tr_b16 v[224:225], v15 offset:57344\n"
    "ds_read_b64_tr_b16 v[226:227], v15 offset:58368\n"
    "v_mfma_f32_32x32x16_bf16 v[144:159], v[132:135], v[236:239], v[144:159]\n"
    "ds_read_b64_tr_b16 v[228:229], v15 offset:59392\n"
    "ds_read_b64_tr_b16 v[230:231], v15 offset:60416\n"
    "buffer_load_dwordx4 v[168:171], v42, s[32:35], s14 offen offset:0 sc1\n"
    "s_waitcnt lgkmcnt(4)\n"
    "v_mfma_f32_32x32x16_bf16 v[144:159], v[136:139], v[240:243], v[144:159]\n"
    "ds_read_b64_tr_b16 v[232:233], v15 offset:61440\n"
    "ds_read_b64_tr_b16 v[234:235], v15 offset:62464\n"
    "v_mfma_f32_32x32x16_bf16 v[144:159], v[140:143], v[244:247], v[144:159]\n"
    "ds_read_b64_tr_b16 v[236:237], v15 offset:63488\n"
    "ds_read_b64_tr_b16 v[238:239], v15 offset:64512\n"
    "buffer_load_dwordx4 v[172:175], v42, s[32:35], s14 offen offset:1024 sc1\n"
    "s_waitcnt lgkmcnt(4)\n"
    "v_mfma_f32_32x32x16_bf16 v[144:159], v[112:115], v[224:227], v[144:159]\n"
    "ds_read_b128 v[240:243], v29 offset:16384\n"
    "v_mfma_f32_32x32x16_bf16 v[144:159], v[116:119], v[228:231], v[144:159]\n"
    "ds_read_b128 v[244:247], v30 offset:16384\n"
    "buffer_load_dword v12, v52, s[32:35], s14 offen offset:2048 sc1\n"
    "s_waitcnt lgkmcnt(2)\n"
    "v_mfma_f32_32x32x16_bf16 v[144:159], v[120:123], v[232:235], v[144:159]\n"
    "ds_read_b128 v[224:227], v29 offset:24576\n"
    "v_mfma_f32_32x32x16_bf16 v[144:159], v[124:127], v[236:239], v[144:159]\n"
    "ds_read_b128 v[228:231], v30 offset:24576\n"
    "s_waitcnt lgkmcnt(2)\n"
    "v_mfma_f32_32x32x16_bf16 v[144:159], a[176:179], v[240:243], v[144:159]\n"
    "ds_read_b128 v[232:235], v31 offset:16384\n"
    "v_mfma_f32_32x32x16_bf16 v[144:159], a[180:183], v[244:247], v[144:159]\n"
    "ds_read_b128 v[236:239], v32 offset:16384\n"
    "s_waitcnt lgkmcnt(2)\n"
    "v_mfma_f32_32x32x16_bf16 v[144:159], a[184:187], v[224:227], v[144:159]\n"
    "ds_read_b128 v[240:243], v31 offset:24576\n"
    "s_waitcnt vmcnt(3)\n"
    "v_sub_u32 v67, s86, v11\n"
    "v_add3_u32 v67, v160, v161, v67\n"
    "v_add3_u32 v67, v162, v163, v67\n"
    "v_add3_u32 v67, v164, v165, v67\n"
    "v_add3_u32 v67, v166, v167, v67\n"
    "s_waitcnt vmcnt(0)\n"
    "v_sub_u32 v4, s86, v12\n"
    "v_mfma_f32_32x32x16_bf16 v[144:159], a[188:191], v[228:231], v[144:159]\n"
    "ds_read_b128 v[244:247], v32 offset:24576\n"
    "v_add3_u32 v4, v168, v169, v4\n"
    "v_add3_u32 v4, v170, v171, v4\n"
    "v_add3_u32 v4, v172, v173, v4\n"
    "v_add3_u32 v4, v174, v175, v4\n"
    "v_or_b32 v67, v67, v4\n"
    "v_cmp_ne_u32 vcc, 0, v67\n"
    "s_nop 0\n"
    "s_cmp_eq_u64 vcc, 0\n"
    "s_cbranch_scc0 .Lsl7\n"
    ".Lok7:\n"
    "s_waitcnt lgkmcnt(2)\n"
    "v_mfma_f32_32x32x16_bf16 v[144:159], a[208:211], v[232:235], v[144:159]\n"
    "ds_read_b128 v[224:227], v33 offset:16384\n"
    "v_lshlrev_b32 v1, 16, v160\n"
    "v_add_f32 v192, v192, v1\n"
    "v_lshlrev_b32 v1, 16, v168\n"
    "v_add_f32 v192, v192, v1\n"
    "v_and_b32 v1, 0xffff0000, v160\n"
    "v_add_f32 v193, v193, v1\n"
    "v_mfma_f32_32x32x16_bf16 v[144:159], a[212:215], v[236:239], v[144:159]\n"
    "ds_read_b128 v[228:231], v34 offset:16384\n"
    "v_and_b32 v1, 0xffff0000, v168\n"
    "v_add_f32 v193, v193, v1\n"
    "v_lshlrev_b32 v1, 16, v161\n"
    "v_add_f32 v194, v194, v1\n"
    "v_lshlrev_b32 v1, 16, v169\n"
    "v_add_f32 v194, v194, v1\n"
    "v_and_b32 v1, 0xffff0000, v161\n"
    "s_waitcnt lgkmcnt(2)\n"
    "v_mfma_f32_32x32x16_bf16 v[144:159], a[216:219], v[240:243], v[144:159]\n"
    "ds_read_b128 v[232:235], v33 offset:24576\n"
    "s_xor_b32 s14, s64, 1\n"
    "s_lshl2_add_u32 s14, s64, s14\n"
    "s_lshl_b32 s14, s14, 14\n"
    "v_add_f32 v195, v195, v1\n"
    "v_and_b32 v1, 0xffff0000, v169\n"
    "v_add_f32 v195, v195, v1\n"
    "v_mfma_f32_32x32x16_bf16 v[144:159], a[220:223], v[244:247], v[144:159]\n"
    "ds_read_b128 v[236:239], v34 offset:24576\n"
    "s_add_u32 s14, s14, s13\n"
    "buffer_load_dwordx4 v[176:179], v42, s[32:35], s14 offen offset:0 sc1\n"
    "v_lshlrev_b32 v1, 16, v162\n"
    "v_add_f32 v196, v196, v1\n"
    "v_lshlrev_b32 v1, 16, v170\n"
    "v_add_f32 v196, v196, v1\n"
    "v_and_b32 v1, 0xffff0000, v162\n"
    "s_waitcnt lgkmcnt(2)\n"
    "v_mfma_f32_32x32x16_bf16 v[144:159], a[224:227], v[224:227], v[144:159]\n"
    "ds_read_b128 v[240:243], v35 offset:16384\n"
    "v_add_f32 v197, v197, v1\n"
    "v_and_b32 v1, 0xffff0000, v170\n"
    "v_add_f32 v197, v197, v1\n"
    "v_lshlrev_b32 v1, 16, v163\n"
    "v_add_f32 v198, v198, v1\n"
    "v_lshlrev_b32 v1, 16, v171\n"
    "v_mfma_f32_32x32x16_bf16 v[144:159], a[228:231], v[228:231], v[144:159]\n"
    "ds_read_b128 v[244:247], v36 offset:16384\n"
    "buffer_load_dwordx4 v[180:183], v42, s[32:35], s14 offen offset:1024 sc1\n"
    "v_add_f32 v198, v198, v1\n"
    "v_and_b32 v1, 0xffff0000, v163\n"
    "v_add_f32 v199, v199, v1\n"
    "v_and_b32 v1, 0xffff0000, v171\n"
    "v_add_f32 v199, v199, v1\n"
    "v_lshlrev_b32 v1, 16, v164\n"
    "s_waitcnt lgkmcnt(2)\n"
    "v_mfma_f32_32x32x16_bf16 v[144:159], a[232:235], v[232:235], v[144:159]\n"
    "ds_read_b128 v[224:227], v35 offset:24576\n"
    "v_add_f32 v200, v200, v1\n"
    "v_lshlrev_b32 v1, 16, v172\n"
    "v_add_f32 v200, v200, v1\n"
    "v_and_b32 v1, 0xffff0000, v164\n"
    "v_add_f32 v201, v201, v1\n"
    "v_and_b32 v1, 0xffff0000, v172\n"
    "v_mfma_f32_32x32x16_bf16 v[144:159], a[236:239], v[236:239], v[144:159]\n"
    "ds_read_b128 v[228:231], v36 offset:24576\n"
    "buffer_load_dword v13, v52, s[32:35], s14 offen offset:2048 sc1\n"
    "v_add_f32 v201, v201, v1\n"
    "v_lshlrev_b32 v1, 16, v165\n"
    "v_add_f32 v202, v202, v1\n"
    "v_lshlrev_b32 v1, 16, v173\n"
    "v_add_f32 v202, v202, v1\n"
    "v_and_b32 v1, 0xffff0000, v165\n"
    "s_waitcnt lgkmcnt(2)\n"
    "v_mfma_f32_32x32x16_bf16 v[144:159], a[240:243], v[240:243], v[144:159]\n"
    "v_add_f32 v203, v203, v1\n"
    "v_and_b32 v1, 0xffff0000, v173\n"
    "v_add_f32 v203, v203, v1\n"
    "v_lshlrev_b32 v1, 16, v166\n"
    "v_add_f32 v204, v204, v1\n"
    "v_lshlrev_b32 v1, 16, v174\n"
    "v_add_f32 v204, v204, v1\n"
    "v_mfma_f32_32x32x16_bf16 v[144:159], a[244:247], v[244:247], v[144:159]\n"
    "v_and_b32 v1, 0xffff0000, v166\n"
    "v_add_f32 v205, v205, v1\n"
    "v_and_b32 v1, 0xffff0000, v174\n"
    "v_add_f32 v205, v205, v1\n"
    "v_lshlrev_b32 v1, 16, v167\n"
    "v_add_f32 v206, v206, v1\n"
    "v_lshlrev_b32 v1, 16, v175\n"
    "v_add_f32 v206, v206, v1\n"
    "s_waitcnt lgkmcnt(0)\n"
    "v_mfma_f32_32x32x16_bf16 v[144:159], a[248:251], v[224:227], v[144:159]\n"
    "v_and_b32 v1, 0xffff0000, v167\n"
    "v_add_f32 v207, v207, v1\n"
    "v_and_b32 v1, 0xffff0000, v175\n"
    "v_add_f32 v207, v207, v1\n"
    "v_mfma_f32_32x32x16_bf16 v[144:159], a[252:255], v[228:231], v[144:159]\n"
    "s_nop 11\n"
    "v_pk_add_f32 v[192:193], v[192:193], v[144:145]\n"
    "v_pk_add_f32 v[194:195], v[194:195], v[146:147]\n"
    "v_pk_add_f32 v[196:197], v[196:197], v[148:149]\n"
    "v_pk_add_f32 v[198:199], v[198:199], v[150:151]\n"
    "v_pk_add_f32 v[200:201], v[200:201], v[152:153]\n"
    "v_pk_add_f32 v[202:203], v[202:203], v[154:155]\n"
    "v_pk_add_f32 v[204:205], v[204:205], v[156:157]\n"
    "v_pk_add_f32 v[206:207], v[206:207], v[158:159]\n"
    "s_waitcnt vmcnt(0)\n"
    "v_sub_u32 v67, s86, v13\n"
    "v_add3_u32 v67, v176, v177, v67\n"
    "v_add3_u32 v67, v178, v179, v67\n"
    "v_add3_u32 v67, v180, v181, v67\n"
    "v_add3_u32 v67, v182, v183, v67\n"
    "v_cmp_ne_u32 vcc, 0, v67\n"
    "s_nop 0\n"
    "s_cmp_eq_u64 vcc, 0\n"
    "s_cbranch_scc0 .Lsl8\n"
    ".Lok8:\n"
    "v_lshlrev_b32 v1, 16, v176\n"
    "v_add_f32 v192, v192, v1\n"
    "buffer_store_dword v192, v37, s[36:39], s12 offen offset:0 sc1\n"
    "v_and_b32 v1, 0xffff0000, v176\n"
    "v_add_f32 v193, v193, v1\n"
    "buffer_store_dword v193, v37, s[36:39], s12 offen offset:2048 sc1\n"
    "s_add_u32 s12, s12, 4096\n"
    "v_lshlrev_b32 v1, 16, v177\n"
    "v_add_f32 v194, v194, v1\n"
    "buffer_store_dword v194, v37, s[36:39], s12 offen offset:0 sc1\n"
    "v_and_b32 v1, 0xffff0000, v177\n"
    "v_add_f32 v195, v195, v1\n"
    "buffer_store_dword v195, v37, s[36:39], s12 offen offset:2048 sc1\n"
    "s_add_u32 s12, s12, 4096\n"
    "v_lshlrev_b32 v1, 16, v178\n"
    "v_add_f32 v196, v196, v1\n"
    "buffer_store_dword v196, v37, s[36:39], s12 offen offset:0 sc1\n"
    "v_and_b32 v1, 0xffff0000, v178\n"
    "v_add_f32 v197, v197, v1\n"
    "buffer_store_dword v197, v37, s[36:39], s12 offen offset:2048 sc1\n"
    "s_add_u32 s12, s12, 4096\n"
    "v_lshlrev_b32 v1, 16, v179\n"
    "v_add_f32 v198, v198, v1\n"
    "buffer_store_dword v198, v37, s[36:39], s12 offen offset:0 sc1\n"
    "v_and_b32 v1, 0xffff0000, v179\n"
    "v_add_f32 v199, v199, v1\n"
    "buffer_store_dword v199, v37, s[36:39], s12 offen offset:2048 sc1\n"
    "s_add_u32 s12, s12, 4096\n"
    "v_lshlrev_b32 v1, 16, v180\n"
    "v_add_f32 v200, v200, v1\n"
    "buffer_store_dword v200, v37, s[36:39], s12 offen offset:0 sc1\n"
    "v_and_b32 v1, 0xffff0000, v180\n"
    "v_add_f32 v201, v201, v1\n"
    "buffer_store_dword v201, v37, s[36:39], s12 offen offset:2048 sc1\n"
    "s_add_u32 s12, s12, 4096\n"
    "v_lshlrev_b32 v1, 16, v181\n"
    "v_add_f32 v202, v202, v1\n"
    "buffer_store_dword v202, v37, s[36:39], s12 offen offset:0 sc1\n"
    "v_and_b32 v1, 0xffff0000, v181\n"
    "v_add_f32 v203, v203, v1\n"
    "buffer_store_dword v203, v37, s[36:39], s12 offen offset:2048 sc1\n"
    "s_add_u32 s12, s12, 4096\n"
    "v_lshlrev_b32 v1, 16, v182\n"
    "v_add_f32 v204, v204, v1\n"
    "buffer_store_dword v204, v37, s[36:39], s12 offen offset:0 sc1\n"
    "v_and_b32 v1, 0xffff0000, v182\n"
    "v_add_f32 v205, v205, v1\n"
    "buffer_store_dword v205, v37, s[36:39], s12 offen offset:2048 sc1\n"
    "s_add_u32 s12, s12, 4096\n"
    "v_lshlrev_b32 v1, 16, v183\n"
    "v_add_f32 v206, v206, v1\n"
    "buffer_store_dword v206, v37, s[36:39], s12 offen offset:0 sc1\n"
    "v_and_b32 v1, 0xffff0000, v183\n"
    "v_add_f32 v207, v207, v1\n"
    "buffer_store_dword v207, v37, s[36:39], s12 offen offset:2048 sc1\n"
    "s_lshl4_add_u32 s68, s60, s62\n"
    "s_cmp_eq_u32 s68, 0\n"
    "s_cselect_b32 s68, 0, 1072693248\n"
    "v_add_u32 v1, s68, v49\n"
    "s_add_u32 s68, s85, 8\n"
    "v_mov_b32 v54, s68\n"
    "s_lshl_b32 s68, s61, 7\n"
    "s_add_u32 s68, s68, 28835840\n"
    "buffer_store_dwordx2 v[54:55], v1, s[32:35], s68 offen sc1\n"
    "s_endpgm\n"
    ".Lsl0:\n"
    "s_add_u32 s90, s90, 1\n"
    "s_cmp_gt_u32 s90, 200000\n"
    "s_cbranch_scc1 .Lok0\n"
    "s_sleep 1\n"
    "buffer_load_dwordx4 v[216:219], v51, s[32:35], 0 offen sc1\n"
    "buffer_load_dwordx4 v[220:223], v51, s[32:35], s87 offen sc1\n"
    "s_waitcnt vmcnt(0)\n"
    "s_branch .Lck0\n"
    ".Lsl1:\n"
    "s_add_u32 s90, s90, 1\n"
    "s_cmp_gt_u32 s90, 200000\n"
    "s_cbranch_scc1 .Lok1\n"
    "s_sleep 1\n"
    "buffer_load_dwordx2 v[74:75], v48, s[32:35], s31 offen sc1\n"
    "s_waitcnt vmcnt(0)\n"
    "s_branch .Lck1\n"
    ".Lsl2:\n"
    "s_add_u32 s90, s90, 1\n"
    "s_cmp_gt_u32 s90, 200000\n"
    "s_cbranch_scc1 .Lok2\n"
    "s_sleep 1\n"
    "buffer_load_dwordx2 v[76:77], v48, s[32:35], s31 offen sc1\n"
    "s_waitcnt vmcnt(0)\n"
    "s_branch .Lck2\n"
    ".Lsl3:\n"
    "s_add_u32 s90, s90, 1\n"
    "s_cmp_gt_u32 s90, 200000\n"
    "s_cbranch_scc1 .Lok3\n"
    "s_sleep 1\n"
    "buffer_load_dwordx2 v[74:75], v48, s[32:35], s31 offen sc1\n"
    "s_waitcnt vmcnt(0)\n"
    "s_branch .Lck3\n"
    ".Lsl4:\n"
    "s_add_u32 s90, s90, 1\n"
    "s_cmp_gt_u32 s90, 200000\n"
    "s_cbranch_scc1 .Lok4\n"
    "s_sleep 1\n"
    "buffer_load_dwordx2 v[74:75], v48, s[32:35], s31 offen sc1\n"
    "s_waitcnt vmcnt(0)\n"
    "s_branch .Lck4\n"
    ".Lsl5:\n"
    "s_add_u32 s90, s90, 1\n"
    "s_cmp_gt_u32 s90, 200000\n"
    "s_cbranch_scc1 .Lok5\n"
    "buffer_load_dwordx4 v[208:211], v42, s[32:35], s45 offen offset:0 sc1\n"
    "buffer_load_dwordx4 v[212:215], v42, s[32:35], s45 offen offset:1024 sc1\n"
    "buffer_load_dwordx4 v[216:219], v42, s[32:35], s45 offen offset:2048 sc1\n"
    "buffer_load_dwordx4 v[220:223], v42, s[32:35], s45 offen offset:3072 sc1\n"
    "buffer_load_dword v11, v52, s[32:35], s94 offen sc1\n"
    "s_waitcnt vmcnt(0)\n"
    "v_sub_u32 v67, s86, v11\n"
    "v_add3_u32 v67, v208, v209, v67\n"
    "v_add3_u32 v67, v210, v211, v67\n"
    "v_add3_u32 v67, v212, v213, v67\n"
    "v_add3_u32 v67, v214, v215, v67\n"
    "v_add3_u32 v67, v216, v217, v67\n"
    "v_add3_u32 v67, v218, v219, v67\n"
    "v_add3_u32 v67, v220, v221, v67\n"
    "v_add3_u32 v67, v222, v223, v67\n"
    "v_cmp_ne_u32 vcc, 0, v67\n"
    "s_nop 0\n"
    "s_cmp_eq_u64 vcc, 0\n"
    "s_cbranch_scc0 .Lsl5\n"
    "s_branch .Lok5\n"
    ".Lsl6:\n"
    "s_add_u32 s90, s90, 1\n"
    "s_cmp_gt_u32 s90, 200000\n"
    "s_cbranch_scc1 .Lok6\n"
    "buffer_load_dwordx4 v[128:131], v42, s[32:35], s46 offen offset:0 sc1\n"
    "buffer_load_dwordx4 v[132:135], v42, s[32:35], s46 offen offset:1024 sc1\n"
    "buffer_load_dwordx4 v[136:139], v42, s[32:35], s46 offen offset:2048 sc1\n"
    "buffer_load_dwordx4 v[140:143], v42, s[32:35], s46 offen offset:3072 sc1\n"
    "buffer_load_dword v12, v52, s[32:35], s95 offen sc1\n"
    "buffer_load_dwordx4 v[112:115], v42, s[32:35], s47 offen offset:0 sc1\n"
    "buffer_load_dwordx4 v[116:119], v42, s[32:35], s47 offen offset:1024 sc1\n"
    "buffer_load_dwordx4 v[120:123], v42, s[32:35], s47 offen offset:2048 sc1\n"
    "buffer_load_dwordx4 v[124:127], v42, s[32:35], s47 offen offset:3072 sc1\n"
    "buffer_load_dword v13, v52, s[32:35], s96 offen sc1\n"
    "s_waitcnt vmcnt(0)\n"
    "v_sub_u32 v67, s86, v12\n"
    "v_add3_u32 v67, v128, v129, v67\n"
    "v_add3_u32 v67, v130, v131, v67\n"
    "v_add3_u32 v67, v132, v133, v67\n"
    "v_add3_u32 v67, v134, v135, v67\n"
    "v_add3_u32 v67, v136, v137, v67\n"
    "v_add3_u32 v67, v138, v139, v67\n"
    "v_add3_u32 v67, v140, v141, v67\n"
    "v_add3_u32 v67, v142, v143, v67\n"
    "v_sub_u32 v4, s86, v13\n"
    "v_add3_u32 v4, v112, v113, v4\n"
    "v_add3_u32 v4, v114, v115, v4\n"
    "v_add3_u32 v4, v116, v117, v4\n"
    "v_add3_u32 v4, v118, v119, v4\n"
    "v_add3_u32 v4, v120, v121, v4\n"
    "v_add3_u32 v4, v122, v123, v4\n"
    "v_add3_u32 v4, v124, v125, v4\n"
    "v_add3_u32 v4, v126, v127, v4\n"
    "v_or_b32 v67, v67, v4\n"
    "v_cmp_ne_u32 vcc, 0, v67\n"
    "s_nop 0\n"
    "s_cmp_eq_u64 vcc, 0\n"
    "s_cbranch_scc0 .Lsl6\n"
    "s_branch .Lok6\n"
    ".Lsl7:\n"
    "s_add_u32 s90, s90, 1\n"
    "s_cmp_gt_u32 s90, 200000\n"
    "s_cbranch_scc1 .Lok7\n"
    "s_xor_b32 s14, s64, 2\n"
    "s_lshl2_add_u32 s14, s64, s14\n"
    "s_lshl_b32 s14, s14, 14\n"
    "s_add_u32 s14, s14, s13\n"
    "buffer_load_dwordx4 v[160:163], v42, s[32:35], s14 offen offset:0 sc1\n"
    "buffer_load_dwordx4 v[164:167], v42, s[32:35], s14 offen offset:1024 sc1\n"
    "buffer_load_dword v11, v52, s[32:35], s14 offen offset:2048 sc1\n"
    "s_xor_b32 s14, s64, 3\n"
    "s_lshl2_add_u32 s14, s64, s14\n"
    "s_lshl_b32 s14, s14, 14\n"
    "s_add_u32 s14, s14, s13\n"
    "buffer_load_dwordx4 v[168:171], v42, s[32:35], s14 offen offset:0 sc1\n"
    "buffer_load_dwordx4 v[172:175], v42, s[32:35], s14 offen offset:1024 sc1\n"
    "buffer_load_dword v12, v52, s[32:35], s14 offen offset:2048 sc1\n"
    "s_waitcnt vmcnt(0)\n"
    "v_sub_u32 v67, s86, v11\n"
    "v_add3_u32 v67, v160, v161, v67\n"
    "v_add3_u32 v67, v162, v163, v67\n"
    "v_add3_u32 v67, v164, v165, v67\n"
    "v_add3_u32 v67, v166, v167, v67\n"
    "v_sub_u32 v4, s86, v12\n"
    "v_add3_u32 v4, v168, v169, v4\n"
    "v_add3_u32 v4, v170, v171, v4\n"
    "v_add3_u32 v4, v172, v173, v4\n"
    "v_add3_u32 v4, v174, v175, v4\n"
    "v_or_b32 v67, v67, v4\n"
    "v_cmp_ne_u32 vcc, 0, v67\n"
    "s_nop 0\n"
    "s_cmp_eq_u64 vcc, 0\n"
    "s_cbranch_scc0 .Lsl7\n"
    "s_branch .Lok7\n"
    ".Lsl8:\n"
    "s_add_u32 s90, s90, 1\n"
    "s_cmp_gt_u32 s90, 200000\n"
    "s_cbranch_scc1 .Lok8\n"
    "s_xor_b32 s14, s64, 1\n"
    "s_lshl2_add_u32 s14, s64, s14\n"
    "s_lshl_b32 s14, s14, 14\n"
    "s_add_u32 s14, s14, s13\n"
    "buffer_load_dwordx4 v[176:179], v42, s[32:35], s14 offen offset:0 sc1\n"
    "buffer_load_dwordx4 v[180:183], v42, s[32:35], s14 offen offset:1024 sc1\n"
    "buffer_load_dword v13, v52, s[32:35], s14 offen offset:2048 sc1\n"
    "s_waitcnt vmcnt(0)\n"
    "v_sub_u32 v67, s86, v13\n"
    "v_add3_u32 v67, v176, v177, v67\n"
    "v_add3_u32 v67, v178, v179, v67\n"
    "v_add3_u32 v67, v180, v181, v67\n"
    "v_add3_u32 v67, v182, v183, v67\n"
    "v_cmp_ne_u32 vcc, 0, v67\n"
    "s_nop 0\n"
    "s_cmp_eq_u64 vcc, 0\n"
    "s_cbranch_scc0 .Lsl8\n"
    "s_branch .Lok8\n"
    ".Lcb:\n"
    :
    : "{s[0:1]}"(__builtin_amdgcn_kernarg_segment_ptr()), "{s2}"(blockIdx.x), "{v0}"(threadIdx.x),
      "i"((__attribute__((address_space(3))) char*)smem)
    : "v1", "v2", "v3", "v4", "v5", "v6", "v7", "v8", "v9", "v10", "v11", "v12", "v13", "v14", "v15", "v16", "v17", "v18", "v19", "v20", "v21", "v22", "v23", "v24", "v25", "v26", "v27", "v28", "v29", "v30", "v31", "v32", "v33", "v34", "v35", "v36", "v37", "v38", "v39", "v40", "v41", "v42", "v43", "v44", "v45", "v46", "v47", "v48", "v49", "v50", "v51", "v52", "v53", "v54", "v55", "v56", "v57", "v58", "v59", "v60", "v61", "v62", "v63", "v64", "v65", "v66", "v67", "v68", "v69", "v70", "v71", "v72", "v73", "v74", "v75", "v76", "v77", "v78", "v79", "v80", "v81", "v82", "v83", "v84", "v85", "v86", "v87", "v88", "v89", "v90", "v91", "v92", "v93", "v94", "v95", "v96", "v97", "v98", "v99", "v100", "v101", "v102", "v103", "v104", "v105", "v106", "v107", "v108", "v109", "v110", "v111", "v112", "v113", "v114", "v115", "v116", "v117", "v118", "v119", "v120", "v121", "v122", "v123", "v124", "v125", "v126", "v127", "v128", "v129", "v130", "v131", "v132", "v133", "v134", "v135", "v136", "v137", "v138", "v139", "v140", "v141", "v142", "v143", "v144", "v145", "v146", "v147", "v148", "v149", "v150", "v151", "v152", "v153", "v154", "v155", "v156", "v157", "v158", "v159", "v160", "v161", "v162", "v163", "v164", "v165", "v166", "v167", "v168", "v169", "v170", "v171", "v172", "v173", "v174", "v175", "v176", "v177", "v178", "v179", "v180", "v181", "v182", "v183", "v184", "v185", "v186", "v187", "v188", "v189", "v190", "v191", "v192", "v193", "v194", "v195", "v196", "v197", "v198", "v199", "v200", "v201", "v202", "v203", "v204", "v205", "v206", "v207", "v208", "v209", "v210", "v211", "v212", "v213", "v214", "v215", "v216", "v217", "v218", "v219", "v220", "v221", "v222", "v223", "v224", "v225", "v226", "v227", "v228", "v229", "v230", "v231", "v232", "v233", "v234", "v235", "v236", "v237", "v238", "v239", "v240", "v241", "v242", "v243", "v244", "v245", "v246", "v247", "v248", "v249", "v250", "v251", "v252", "v253", "v254", "v255", "a0", "a1", "a2", "a3", "a4", "a5", "a6", "a7", "a8", "a9", "a10", "a11", "a12", "a13", "a14", "a15", "a16", "a17", "a18", "a19", "a20", "a21", "a22", "a23", "a24", "a25", "a26", "a27", "a28", "a29", "a30", "a31", "a32", "a33", "a34", "a35", "a36", "a37", "a38", "a39", "a40", "a41", "a42", "a43", "a44", "a45", "a46", "a47", "a48", "a49", "a50", "a51", "a52", "a53", "a54", "a55", "a56", "a57", "a58", "a59", "a60", "a61", "a62", "a63", "a64", "a65", "a66", "a67", "a68", "a69", "a70", "a71", "a72", "a73", "a74", "a75", "a76", "a77", "a78", "a79", "a80", "a81", "a82", "a83", "a84", "a85", "a86", "a87", "a88", "a89", "a90", "a91", "a92", "a93", "a94", "a95", "a96", "a97", "a98", "a99", "a100", "a101", "a102", "a103", "a104", "a105", "a106", "a107", "a108", "a109", "a110", "a111", "a112", "a113", "a114", "a115", "a116", "a117", "a118", "a119", "a120", "a121", "a122", "a123", "a124", "a125", "a126", "a127", "a128", "a129", "a130", "a131", "a132", "a133", "a134", "a135", "a136", "a137", "a138", "a139", "a140", "a141", "a142", "a143", "a144", "a145", "a146", "a147", "a148", "a149", "a150", "a151", "a152", "a153", "a154", "a155", "a156", "a157", "a158", "a159", "a160", "a161", "a162", "a163", "a164", "a165", "a166", "a167", "a168", "a169", "a170", "a171", "a172", "a173", "a174", "a175", "a176", "a177", "a178", "a179", "a180", "a181", "a182", "a183", "a184", "a185", "a186", "a187", "a188", "a189", "a190", "a191", "a192", "a193", "a194", "a195", "a196", "a197", "a198", "a199", "a200", "a201", "a202", "a203", "a204", "a205", "a206", "a207", "a208", "a209", "a210", "a211", "a212", "a213", "a214", "a215", "a216", "a217", "a218", "a219", "a220", "a221", "a222", "a223", "a224", "a225", "a226", "a227", "a228", "a229", "a230", "a231", "a232", "a233", "a234", "a235", "a236", "a237", "a238", "a239", "a240", "a241", "a242", "a243", "a244", "a245", "a246", "a247", "a248", "a249", "a250", "a251", "a252", "a253", "a254", "a255", "s3", "s4", "s5", "s6", "s7", "s8", "s9", "s10", "s11", "s12", "s13", "s14", "s15", "s16", "s17", "s18", "s19", "s20", "s21", "s22", "s23", "s24", "s25", "s26", "s27", "s28", "s29", "s30", "s31", "s33", "s34", "s35", "s36", "s37", "s38", "s39", "s40", "s41", "s42", "s43", "s44", "s45", "s46", "s47", "s48", "s49", "s50", "s51", "s52", "s53", "s54", "s55", "s56", "s57", "s58", "s59", "s60", "s61", "s62", "s63", "s64", "s65", "s66", "s67", "s68", "s69", "s70", "s71", "s72", "s73", "s74", "s75", "s76", "s77", "s78", "s79", "s80", "s81", "s82", "s83", "s84", "s85", "s86", "s87", "s88", "s89", "s90", "s91", "s92", "s93", "s94", "s95", "s96", "s97", "s98", "s99", "s100", "s101", "vcc", "scc", "memory");
}

extern "C" void kernel_launch(void* const* d_in, const int* in_sizes, int n_in, void* d_out, int out_size, void* d_ws,
                              size_t ws_size, hipStream_t stream) {
  const u32 f = 0x00020000u;
  s5_mlp_fused<<<256, 256, 0, stream>>>(d_in[0], 2048u * 512u * 2u, f, d_in[3], 512u * 512u * 2u, f, d_in[4], 512u * 512u * 2u, f,
                                        d_in[10], 512u * 1792u * 2u, f, d_in[12], 1792u * 512u * 2u, f, d_in[5], 512u * 512u * 2u, f,
                                        d_in[6], 512u * 512u * 2u, f, d_ws, 28839936u, f, d_out, 2048u * 512u * 4u, f,
                                        d_in[11], 1792u * 2u, f, d_in[1], d_in[2], d_in[7], d_in[8], d_in[13]);
}
```
